```python
import torch

def _softmax(x, axis=-1):
    return torch.softmax(x, dim=axis)
def _gelu(x):
    return torch.nn.functional.gelu(x, approximate="tanh")
def _silu(x):
    return torch.nn.functional.silu(x)
def _sigmoid(x):
    return torch.sigmoid(x)
def _layernorm(x, axis=-1, eps=1e-5):
    m = x.mean(dim=axis, keepdim=True)
    v = x.var(dim=axis, keepdim=True, unbiased=False)
    return (x - m) / torch.sqrt(v + eps)
def _scatter_add(tgt, idx, src, axis=0):
    return tgt.clone().index_add_(axis, idx, src)
def _segment_sum(src, seg, n):
    out = torch.zeros((n,) + src.shape[1:], dtype=src.dtype)
    return out.index_add_(0, seg, src)
def _pad(x, pw):
    flat = tuple(v for pair in reversed(pw) for v in pair)
    return torch.nn.functional.pad(x, flat)

kernel_name = 'parallel_sum__sliding_block_cached__mixer_channel_mlp__decode'

def setup_inputs():

    _w = (lambda a: (lambda t: t.to(torch.bfloat16) if t.is_floating_point() else t)(torch.as_tensor(a)))
    def _A_setup():
        B, Sq, D = 1, 1, 576
        Hq, Hkv, Sk, Dh = 32, 16, 16384, 128
        assert Hq % Hkv == 0, (Hq, Hkv)
        return {
            "x": _w((lambda *s: torch.randn(*s, dtype=torch.bfloat16) * 0.05)(B, Sq, D)),
            "k_cache": _w((lambda *s: torch.randn(*s, dtype=torch.bfloat16) * 3.2)(B, Hkv, Sk, Dh)),
            "v_cache": _w((lambda *s: torch.randn(*s, dtype=torch.bfloat16) * 0.4)(B, Hkv, Sk, Dh)),
            "wq": _w((lambda *s: torch.randn(*s, dtype=torch.bfloat16) * 3.2)(D, Hq * Dh) * (D ** -0.5)),
            "wo": _w((lambda *s: torch.randn(*s, dtype=torch.bfloat16) * 0.4)(Hq * Dh, D) * ((Hq * Dh) ** -0.5)),
        }
    _A_in = _A_setup()

    def _B_setup():
        B, S, D, F = 1, 1, 576, 4608
        return {
            "x": _w((lambda *s: torch.randn(*s, dtype=torch.bfloat16) * 0.05)(B, S, D)),
            "w1": _w((lambda *s: torch.randn(*s, dtype=torch.bfloat16) * 3.2)(D, F) * (D ** -0.5)),
            "b1": _w((lambda *s: torch.randn(*s, dtype=torch.bfloat16) * 1.6)(F)),
            "w2": _w((lambda *s: torch.randn(*s, dtype=torch.bfloat16) * 0.1)(F, D) * (F ** -0.5)),
            "b2": _w((lambda *s: torch.randn(*s, dtype=torch.bfloat16) * 0.1)(D)),
        }
    _B_in = _B_setup()

    return {
        "A_x": _A_in["x"],
        "A_k_cache": _A_in["k_cache"],
        "A_v_cache": _A_in["v_cache"],
        "A_wq": _A_in["wq"],
        "A_wo": _A_in["wo"],
        "B_x": _B_in["x"],
        "B_w1": _B_in["w1"],
        "B_b1": _B_in["b1"],
        "B_w2": _B_in["w2"],
        "B_b2": _B_in["b2"],
    }

def reference(A_x, A_k_cache, A_v_cache, A_wq, A_wo, B_x, B_w1, B_b1, B_w2, B_b2):
    def _A_ref(x, k_cache, v_cache, wq, wo):
        B, Sq, D = x.shape
        Hq, Dh = 32, 128
        Hkv, Sk = k_cache.shape[1], k_cache.shape[2]
        W = min(4096, max(256, Sk // 4))
        rep = Hq // Hkv
        q = (lambda x, *d: x.permute(*d))((x @ wq).reshape(B, Sq, Hq, Dh), 0, 2, 1, 3)
        k = (lambda x, n, axis: torch.repeat_interleave(x, n, dim=axis))(k_cache[:, :, Sk - W:, :], rep, axis=1)
        v = (lambda x, n, axis: torch.repeat_interleave(x, n, dim=axis))(v_cache[:, :, Sk - W:, :], rep, axis=1)
        scores = torch.einsum("bhqd,bhkd->bhqk", q, k) * (Dh ** -0.5)
        p = _softmax(scores, axis=-1)
        o = torch.einsum("bhqk,bhkd->bhqd", p, v)
        return (lambda x, *d: x.permute(*d))(o, 0, 2, 1, 3).reshape(B, Sq, Hq * Dh) @ wo

    def _B_ref(x, w1, b1, w2, b2):
        y = _layernorm(x, axis=-1)
        h = _gelu(torch.einsum("bsd,df->bsf", y, w1) + b1)
        return torch.einsum("bsf,fd->bsd", h, w2) + b2

    x = A_x
    xn = _layernorm(x, axis=-1)
    return x + _A_ref(xn, A_k_cache, A_v_cache, A_wq, A_wo) + _B_ref(xn, B_w1, B_b1, B_w2, B_b2)
```

```cpp
#include <hip/hip_runtime.h>
#include <hip/hip_bf16.h>
#include <stdint.h>

#define MIMIC 1

typedef unsigned short u16;
__device__ __forceinline__ float bf2f(u16 v) { return __uint_as_float(((unsigned)v) << 16); }
__device__ __forceinline__ u16 f2bf(float f) { unsigned u = __float_as_uint(f); return (u16)((u + 0x7fffu + ((u >> 16) & 1u)) >> 16); }
__device__ __forceinline__ float rb(float f) {
  return bf2f(f2bf(f));
}

constexpr int D = 576, HQ = 32, HKV = 16, SK = 16384, DH = 128, W = 4096, F = 4608;

__device__ float block_sum(float v, float* red) {
  for (int o = 32; o > 0; o >>= 1) v += __shfl_xor(v, o);
  __syncthreads();
  if ((threadIdx.x & 63) == 0) red[threadIdx.x >> 6] = v;
  __syncthreads();
  float s = 0; for (int i = 0; i < (int)(blockDim.x >> 6); ++i) s += red[i];
  return s;
}
__device__ float block_max(float v, float* red) {
  for (int o = 32; o > 0; o >>= 1) v = fmaxf(v, __shfl_xor(v, o));
  __syncthreads();
  if ((threadIdx.x & 63) == 0) red[threadIdx.x >> 6] = v;
  __syncthreads();
  float s = -INFINITY; for (int i = 0; i < (int)(blockDim.x >> 6); ++i) s = fmaxf(s, red[i]);
  return s;
}
__device__ void ln(const float* in, float* out, float* red) {
  float s = 0; for (int i = threadIdx.x; i < D; i += blockDim.x) s += in[i];
  float mean = block_sum(s, red) / D;
  float q = 0; for (int i = threadIdx.x; i < D; i += blockDim.x) { float d = in[i] - mean; q += d * d; }
  float var = block_sum(q, red) / D;
  float mb = rb(mean), vb = rb(var);
  float den = rb(sqrtf(rb(vb + 1e-5f)));
  for (int i = threadIdx.x; i < D; i += blockDim.x) out[i] = rb(rb(in[i] - mb) / den);
  __syncthreads();
}

__global__ void k1(const u16* x, const u16* wq, const u16* w1, const u16* b1, float* q, float* h) {
  __shared__ float xs[D], xn[D], y[D], red[16];
  for (int i = threadIdx.x; i < D; i += blockDim.x) xs[i] = bf2f(x[i]);
  __syncthreads();
  ln(xs, xn, red);
  ln(xn, y, red);
  int c = blockIdx.x * 256 + threadIdx.x;
  if (c < HQ * DH) {
    float a = 0; for (int k = 0; k < D; ++k) a += xn[k] * bf2f(wq[(size_t)k * (HQ * DH) + c]);
    q[c] = rb(a);
  } else {
    c -= HQ * DH;
    if (c < F) {
      float a = 0; for (int k = 0; k < D; ++k) a += y[k] * bf2f(w1[(size_t)k * F + c]);
      a = rb(rb(a) + bf2f(b1[c]));
      float t = tanhf(0.7978845608028654f * (a + 0.044715f * a * a * a));
      h[c] = rb(0.5f * a * (1.f + t));
    }
  }
}
__global__ void k2(const u16* kc, const u16* vc, const float* q, float* o) {
  __shared__ float sc[W], qs[DH], red[16];
  int hq = blockIdx.x, hk = hq / 2;
  if (threadIdx.x < DH) qs[threadIdx.x] = q[hq * DH + threadIdx.x];
  __syncthreads();
  const u16* kb = kc + ((size_t)hk * SK + (SK - W)) * DH;
  const u16* vb = vc + ((size_t)hk * SK + (SK - W)) * DH;
  float m = -INFINITY;
  for (int j = threadIdx.x; j < W; j += blockDim.x) {
    float a = 0; for (int d = 0; d < DH; ++d) a += qs[d] * bf2f(kb[(size_t)j * DH + d]);
    a = rb(rb(a) * 0.08838834764831845f);
    sc[j] = a; m = fmaxf(m, a);
  }
  m = block_max(m, red);
  float s = 0;
  for (int j = threadIdx.x; j < W; j += blockDim.x) { float e = expf(sc[j] - m); sc[j] = e; s += e; }
  s = block_sum(s, red);
  for (int j = threadIdx.x; j < W; j += blockDim.x) sc[j] = rb(sc[j] / s);
  __syncthreads();
  if (threadIdx.x < DH) {
    float a = 0; for (int j = 0; j < W; ++j) a += sc[j] * bf2f(vb[(size_t)j * DH + threadIdx.x]);
    o[hq * DH + threadIdx.x] = rb(a);
  }
}
__global__ void k3(const u16* x, const u16* wo, const u16* w2, const u16* b2, const float* o, const float* h, u16* out) {
  __shared__ float red[16];
  int c = blockIdx.x;
  float a = 0; for (int k = threadIdx.x; k < HQ * DH; k += blockDim.x) a += o[k] * bf2f(wo[(size_t)k * D + c]);
  float b = 0; for (int k = threadIdx.x; k < F; k += blockDim.x) b += h[k] * bf2f(w2[(size_t)k * D + c]);
  a = block_sum(a, red); b = block_sum(b, red);
  if (threadIdx.x == 0) {
    float r = rb(bf2f(x[c]) + rb(a)) + rb(rb(b) + bf2f(b2[c]));
    out[c] = f2bf(r);
  }
}

extern "C" void kernel_launch(void* const* d_in, const int* in_sizes, int n_in, void* d_out, int out_size, void* d_ws, size_t ws_size, hipStream_t stream) {
  const u16* x = (const u16*)d_in[0]; const u16* kc = (const u16*)d_in[1]; const u16* vc = (const u16*)d_in[2];
  const u16* wq = (const u16*)d_in[3]; const u16* wo = (const u16*)d_in[4];
  const u16* w1 = (const u16*)d_in[6]; const u16* b1 = (const u16*)d_in[7]; const u16* w2 = (const u16*)d_in[8]; const u16* b2 = (const u16*)d_in[9];
  float* q = (float*)d_ws; float* h = q + 4096; float* o = h + 4608;
  k1<<<(4096 + 4608) / 256, 256, 0, stream>>>(x, wq, w1, b1, q, h);
  k2<<<32, 256, 0, stream>>>(kc, vc, q, o);
  k3<<<576, 256, 0, stream>>>(x, wo, w2, b2, o, h, (u16*)d_out);
}
```

```cpp
#include <hip/hip_runtime.h>
#include <hip/hip_bf16.h>
#include <stdint.h>

typedef unsigned short u16;
typedef unsigned long long u64;
typedef __attribute__((ext_vector_type(8))) __bf16 bf16x8;
typedef __attribute__((ext_vector_type(2))) __bf16 bf16x2;
typedef __attribute__((ext_vector_type(4))) float f32x4;
typedef __attribute__((ext_vector_type(2))) float f32x2;
typedef __attribute__((ext_vector_type(4))) unsigned u32x4;
typedef __attribute__((ext_vector_type(2))) unsigned u32x2;
#define LAS __attribute__((address_space(3)))

#define AUX_W 2
#define WIN_A 3
#define WIN_W 8
#define GATE_AT 15
#define QDELAY 0
#define AUX_SC1 16

constexpr int D = 576, NQ = 4096, SK = 16384, DH = 128, WIN = 4096, F = 4608;
constexpr int NWAVE = 12, NTHR = NWAVE * 64;
constexpr int R_W = 1  , R_ATT = 4  ;
constexpr int WS_E = 0;
constexpr int WS_QG = 4096;
constexpr int WS_HG = WS_QG + 16 * 4 * 128 * 16;
constexpr int WS_OG = WS_HG + 18 * 2304 * 16;
constexpr int WS_ML = WS_OG + 256 * 16 * 8 * 16;
constexpr int WS_FG = WS_ML + 16 * 2 * 16 * 16;
constexpr int WS_END = WS_FG + 256 * 288 * 16;
constexpr int WS_DBG = 8 << 20;
constexpr int OOB = 0x7ff00000;
constexpr unsigned SPIN_CAP = 1u << 20;
enum { FL_Q = 0, FL_ISS = 1, FL_ARR = 2, FL_OPUB = 3, FL_A = 4, FL_OUT = 5, FL_TAG = 6 };
constexpr int L_FLAG = 0;
constexpr int L_Q = 64;
constexpr int L_X = L_Q + 512;
constexpr int L_Y = L_X + 2304;
constexpr int L_ML = L_Y + 2304;
constexpr int L_AO = L_ML + 128;
constexpr int L_AH = L_AO + 64;
constexpr int L_PART = L_AH + 128;
constexpr int L_END = L_PART + 8 * 1024;

#define RSRC(p, n) __builtin_amdgcn_make_buffer_rsrc((void*)(p), 0, (n), 0x00020000)
#define BLD128(r, off, soff, aux) __builtin_amdgcn_raw_buffer_load_b128((r), (off), (soff), (aux))
#define BST128(v, r, off, aux) __builtin_amdgcn_raw_buffer_store_b128((v), (r), (off), 0, (aux))
#define CFENCE() asm volatile("" ::: "memory")
#define LDS_WAIT() asm volatile("s_waitcnt lgkmcnt(0)" ::: "memory")
#define KEEP(v) asm volatile("" :: "v"(v))
#define SB() __builtin_amdgcn_sched_barrier(0)
#define LDS_INC(i) __hip_atomic_fetch_add((LAS unsigned*)(flag + (i)), 1u, __ATOMIC_RELAXED, __HIP_MEMORY_SCOPE_WORKGROUP)
#define TAGOK(v) (((v)[1] == tag) & ((v)[3] == tag))
constexpr float LOG2E = 1.4426950408889634f;

#define TS(i)
#define TS_FLUSH()

__device__ __forceinline__ float asf(unsigned u) { return __builtin_bit_cast(float, u); }
__device__ __forceinline__ unsigned asu(float f) { return __builtin_bit_cast(unsigned, f); }
__device__ __forceinline__ unsigned cvtpk(float lo, float hi) {
  bf16x2 r = {(__bf16)lo, (__bf16)hi};
  return __builtin_bit_cast(unsigned, r);
}
__device__ __forceinline__ float lo16(unsigned u) { return asf(u << 16); }
__device__ __forceinline__ float hi16(unsigned u) { return asf(u & 0xffff0000u); }
__device__ __forceinline__ float rb(float f) { return hi16(cvtpk(f, f)); }
template <int CTRL> __device__ __forceinline__ float dpp(float x) {
  return asf(__builtin_amdgcn_mov_dpp(asu(x), CTRL, 0xf, 0xf, true));
}
constexpr int XOR1 = 0xB1, XOR2 = 0x4E, HMIR = 0x141, MIR = 0x140, ROR8 = 0x128;
__device__ __forceinline__ float sum8(float x) { x += dpp<XOR1>(x); x += dpp<XOR2>(x); x += dpp<HMIR>(x); return x; }
__device__ __forceinline__ float max8(float x) { x = fmaxf(x, dpp<XOR1>(x)); x = fmaxf(x, dpp<XOR2>(x)); x = fmaxf(x, dpp<HMIR>(x)); return x; }
__device__ __forceinline__ float xrow_sum(float x) {
  auto s = __builtin_amdgcn_permlane16_swap(asu(x), asu(x), false, false);
  x = asf(s[0]) + asf(s[1]);
  auto t = __builtin_amdgcn_permlane32_swap(asu(x), asu(x), false, false);
  return asf(t[0]) + asf(t[1]);
}
__device__ __forceinline__ float xrow_max(float x) {
  auto s = __builtin_amdgcn_permlane16_swap(asu(x), asu(x), false, false);
  x = fmaxf(asf(s[0]), asf(s[1]));
  auto t = __builtin_amdgcn_permlane32_swap(asu(x), asu(x), false, false);
  return fmaxf(asf(t[0]), asf(t[1]));
}
__device__ __forceinline__ float wave_sum(float x) { x = sum8(x); x += dpp<MIR>(x); return xrow_sum(x); }
__device__ __forceinline__ f32x4 mfma(u32x4 a, u32x4 b, f32x4 c) {
  return __builtin_amdgcn_mfma_f32_16x16x32_bf16(__builtin_bit_cast(bf16x8, a), __builtin_bit_cast(bf16x8, b), c, 0, 0, 0);
}
__device__ __forceinline__ float dot8(u32x4 a, u32x4 b) {
  float d = 0.f;
#pragma unroll
  for (int k = 0; k < 4; ++k) { const unsigned ak = a[k], bk = b[k]; d = __builtin_amdgcn_fdot2_f32_bf16(__builtin_bit_cast(bf16x2, ak), __builtin_bit_cast(bf16x2, bk), d, false); }
  return d;
}
__device__ __forceinline__ f32x2 fold_pair(f32x4 acc) {
  unsigned u[4];
#pragma unroll
  for (int r = 0; r < 4; ++r) u[r] = __builtin_amdgcn_update_dpp(asu(acc[r]), asu(acc[r]), ROR8, 0xc, 0xf, false);
  auto s0 = __builtin_amdgcn_permlane32_swap(u[0], u[2], false, false);
  auto s1 = __builtin_amdgcn_permlane32_swap(u[1], u[3], false, false);
  return f32x2{asf(s0[0]) + asf(s0[1]), asf(s1[0]) + asf(s1[1])};
}
__device__ __forceinline__ void lds_wait_ge(volatile LAS unsigned* p, unsigned v) {
  _Pragma("nounroll") for (unsigned i = 0; *p < v && i < SPIN_CAP; ++i) __builtin_amdgcn_s_sleep(1);
  CFENCE();
}
__device__ __forceinline__ void layernorm(float (&v)[12], unsigned (&d)[12], bool act) {
  float s = 0.f;
#pragma unroll
  for (int i = 0; i < 12; i += 4) s += (v[i] + v[i + 1]) + (v[i + 2] + v[i + 3]);
  const float mean = wave_sum(s) * (1.f / D);
  float q0 = 0.f, q1 = 0.f;
#pragma unroll
  for (int i = 0; i < 12; i += 2) { const float a = v[i] - mean, c = v[i + 1] - mean; q0 = fmaf(a, a, q0); q1 = fmaf(c, c, q1); }
  const float var = wave_sum(act ? q0 + q1 : 0.f) * (1.f / D);
  const float mb = rb(mean);
  const float den = rb(__builtin_amdgcn_sqrtf(rb(rb(var) + 1e-5f)));
  const float inv = act ? __builtin_amdgcn_rcpf(den) : 0.f;
#pragma unroll
  for (int i = 0; i < 12; i += 2) {
    const unsigned t = cvtpk(v[i] - mb, v[i + 1] - mb);
    const float a = lo16(t) * inv, c = hi16(t) * inv;
    d[i] = cvtpk(a, a); d[i + 1] = cvtpk(c, c);
    v[i] = hi16(d[i]); v[i + 1] = hi16(d[i + 1]);
  }
}

__global__ void __launch_bounds__(NTHR) fused_k(const u16* x, const u16* wq, const u16* kc, const u16* vc, unsigned char* ws,
                                                const u16* w1, const u16* w2, const u16* wo, const u16* b1, const u16* b2, u16* out) {
  __shared__ __attribute__((aligned(16))) unsigned char lds_raw[L_END];
  LAS unsigned char* lds = (LAS unsigned char*)lds_raw;
  volatile LAS unsigned* flag = (volatile LAS unsigned*)(lds + L_FLAG);
  const int b = blockIdx.x, tid = threadIdx.x, w = __builtin_amdgcn_readfirstlane(tid >> 6), l = tid & 63;
  const int g = (b & 7) * 2 + (b >> 7), s = (b >> 3) & 15;
  const int rho = l >> 3, c8 = l & 7, h4 = l >> 4, c16 = l & 15;
  TS(0);
  auto rws = RSRC(ws, WS_END);
  u32x4 msk;
#pragma unroll
  for (int r = 0; r < 4; ++r) msk[r] = ((c8 >> 1) == r) ? ((c8 & 1) ? 0xffff0000u : 0x0000ffffu) : 0u;
  int fp = -1;
  unsigned tag = 0;

  if (w >= R_ATT) {
    const int aw = w - R_ATT;
    u32x4 KV[16];
    {
      const int kvb = (g * SK + (SK - WIN) + s * 256 + aw * 32) * DH * 2;
      const int rot = (((unsigned)(b * 8 + aw) * 0x9E3779B1u) >> 29) << 10;
      auto rk = RSRC(kc, 16 * SK * DH * 2);
      auto rv = RSRC(vc, 16 * SK * DH * 2);
#pragma unroll
      for (int i = 0; i < 16; ++i) {
        if (i >= WIN_A) KEEP(KV[i - WIN_A]);
        KV[i] = BLD128(i < 8 ? rk : rv, l * 16, kvb + (((i & 7) << 10) ^ rot), AUX_W);
        if (i == WIN_A - 1) { __builtin_amdgcn_s_barrier(); TS(1); }
        if (i == GATE_AT) { if (l == 0) LDS_INC(FL_ISS); }
        SB();
      }
    }
    CFENCE();
    TS(2);
    lds_wait_ge(flag + FL_Q, 1);
    const u32x4 qa = *(LAS u32x4*)(lds + L_Q + (c16 >> 3) * 256 + c16 * 16);
    const u32x4 qb = *(LAS u32x4*)(lds + L_Q + ((c16 >> 3) ^ 1) * 256 + c16 * 16);
    TS(3);
    float sc[8], m = -INFINITY;
#pragma unroll
    for (int i = 0; i < 8; ++i) {
      const float t = sum8(dot8(KV[i], qa) + dpp<ROR8>(dot8(KV[i], qb)));
      sc[i] = rb(rb(t) * 0.08838834764831845f);
      m = fmaxf(m, sc[i]);
    }
    m = xrow_max(m);
    float lsum = 0.f;
    unsigned pp[8];
#pragma unroll
    for (int i = 0; i < 8; ++i) { const float p = __builtin_amdgcn_exp2f((sc[i] - m) * LOG2E); lsum += p; pp[i] = cvtpk(p, p); }
    lsum = xrow_sum(lsum);
    if (l == 0 || l == 8) *(LAS f32x2*)(lds + L_ML + (rho * 8 + aw) * 8) = f32x2{m, lsum};
    TS(4);
    f32x4 o0 = {0.f, 0.f, 0.f, 0.f}, o1 = o0;
#pragma unroll
    for (int i = 0; i < 8; i += 2) { o0 = mfma(msk & pp[i], KV[8 + i], o0); o1 = mfma(msk & pp[i + 1], KV[9 + i], o1); }
    *(LAS f32x4*)(lds + L_PART + aw * 1024 + ((h4 >> 1) * 128 + 8 * c16 + 4 * (h4 & 1)) * 4) = o0 + o1;
    LDS_WAIT();
    TS(5);
    unsigned old = 0;
    if (l == 0) old = LDS_INC(FL_ARR);
    old = __builtin_amdgcn_readfirstlane(old);
    tag = __builtin_amdgcn_readfirstlane(flag[FL_TAG]);
    CFENCE();
    if (old == 7) {
      f32x4 ml[4], pa[8];
#pragma unroll
      for (int i = 0; i < 4; ++i) ml[i] = *(LAS f32x4*)(lds + L_ML + (l >> 5) * 64 + i * 16);
#pragma unroll
      for (int i = 0; i < 8; ++i) pa[i] = *(LAS f32x4*)(lds + L_PART + i * 1024 + l * 16);
      float M = -INFINITY;
#pragma unroll
      for (int i = 0; i < 4; ++i) M = fmaxf(M, fmaxf(ml[i][0], ml[i][2]));
      float L = 0.f; f32x4 o = {0.f, 0.f, 0.f, 0.f};
#pragma unroll
      for (int i = 0; i < 8; ++i) {
        const float e = __builtin_amdgcn_exp2f((ml[i >> 1][(i & 1) * 2] - M) * LOG2E);
        L = fmaf(e, ml[i >> 1][(i & 1) * 2 + 1], L);
        o += e * pa[i];
      }
      const int og = WS_OG + (((g * 16 + (l >> 2)) * 16 + s) * 8 + (l & 3)) * 16;
      BST128((u32x4{asu(o[0]), tag, asu(o[1]), tag}), rws, og, AUX_SC1);
      BST128((u32x4{asu(o[2]), tag, asu(o[3]), tag}), rws, og + 64, AUX_SC1);
      BST128((u32x4{asu(M), tag, asu(L), tag}), rws, (l & 31) == 0 ? WS_ML + ((g * 2 + (l >> 5)) * 16 + s) * 16 : OOB, AUX_SC1);
      CFENCE();
      if (l == 0) flag[FL_OPUB] = 1u;
      TS(6);
    }
    if (aw == 0 && b < 32) fp = 256 + b;
  } else if (w >= R_W) {
    const int ow = w - R_W;
    __builtin_amdgcn_s_barrier();
    CFENCE();
    const int p = b + 256 * ow;
    const int part = p / 36, slab = p - part * 36;
    auto r1 = RSRC(w1, p < 648 ? D * F * 2 : 0);
    auto r2 = RSRC(w2, F * D * 2);
    auto ro = RSRC(wo, NQ * D * 2);
    const int o1 = ((part * 32 + 8 * h4) * F + slab * 128 + c16 * 8) * 2;
    const int col = (ow * 192 + c8 * 8) * 2;
    const int o2 = (18 * b + 2 * rho) * D * 2 + col;
    const int o2c = rho < 2 ? (18 * b + 16 + rho) * D * 2 + col : OOB;
    const int oo = (g * 256 + 16 * s + 4 * (rho & 3) + 2 * (rho >> 2)) * D * 2 + col;
    lds_wait_ge(flag + FL_Q, 1);
    tag = __builtin_amdgcn_readfirstlane(flag[FL_TAG]);
    u32x4 av[2];
    av[0] = *(LAS u32x4*)(lds + L_Y + (part * 32 + 8 * h4) * 4);
    av[1] = *(LAS u32x4*)(lds + L_Y + (part * 32 + 8 * h4 + 4) * 4);
    lds_wait_ge(flag + FL_ISS, 8);
    TS(1);
    u32x4 W[23];
    f32x4 acc = {0.f, 0.f, 0.f, 0.f};
#pragma unroll
    for (int i = 0; i < 23 + WIN_W; ++i) {
      const int j = i - WIN_W;
      if (j >= 0 && j < 8) acc = mfma(msk & av[j >> 2][j & 3], W[j], acc);
      else if (j >= 0 && i < 23) KEEP(W[j]);
      if (i < 8) W[i] = BLD128(r1, o1, i * F * 2, AUX_W);
      else if (i < 17) { const int cb = (i - 8) / 3, u = (i - 8) % 3; W[i] = BLD128(r2, (u == 2 ? o2c : o2) + cb * 128 + (u == 1 ? D * 2 : 0), 0, AUX_W); }
      else if (i < 23) { const int cb = (i - 17) >> 1, u = (i - 17) & 1; W[i] = BLD128(ro, oo + cb * 128 + u * D * 2, 0, AUX_W); }
      if (j == 7) {
        const int pair = slab * 64 + 4 * c16 + 2 * (h4 & 1) + (h4 >> 1);
        const float v0 = l < 32 ? acc[0] : acc[2], v1 = l < 32 ? acc[1] : acc[3];
        BST128((u32x4{asu(v0), tag, asu(v1), tag}), rws, p < 648 ? WS_HG + (part * 2304 + pair) * 16 : OOB, AUX_SC1);
        TS(2);
      }
      SB();
    }
    CFENCE();
    TS(3);
    lds_wait_ge(flag + FL_A, 1);
    const u32x2 ah = *(LAS u32x2*)(lds + L_AH + rho * 8), ao = *(LAS u32x2*)(lds + L_AO + rho * 8);
    const unsigned ah2 = *(LAS unsigned*)(lds + L_AH + 64 + (rho & 1) * 4);
    TS(4);
    const u32x4 A0 = msk & ah[0], A1 = msk & ah[1], A2 = msk & ah2, A3 = msk & ao[0], A4 = msk & ao[1];
    f32x4 oc[3];
#pragma unroll
    for (int cb = 0; cb < 3; ++cb) oc[cb] = mfma(A0, W[8 + 3 * cb], f32x4{0.f, 0.f, 0.f, 0.f});
#pragma unroll
    for (int cb = 0; cb < 3; ++cb) oc[cb] = mfma(A1, W[9 + 3 * cb], oc[cb]);
#pragma unroll
    for (int cb = 0; cb < 3; ++cb) oc[cb] = mfma(A2, W[10 + 3 * cb], oc[cb]);
#pragma unroll
    for (int cb = 0; cb < 3; ++cb) oc[cb] = mfma(A3, W[17 + 2 * cb], oc[cb]);
#pragma unroll
    for (int cb = 0; cb < 3; ++cb) oc[cb] = mfma(A4, W[18 + 2 * cb], oc[cb]);
    const int fo = (l & 8) ? OOB : WS_FG + (b * 288 + ow * 96 + 4 * c8 + 2 * (h4 & 1) + (h4 >> 1)) * 16;
#pragma unroll
    for (int cb = 0; cb < 3; ++cb) {
      const f32x2 pr = fold_pair(oc[cb]);
      BST128((u32x4{asu(pr[0]), tag, asu(pr[1]), tag}), rws, fo + cb * 512, AUX_SC1);
    }
    CFENCE();
    if (l == 0) LDS_INC(FL_OUT);
    TS(5);
  } else {
    __builtin_amdgcn_s_setprio(3);
    auto rx = RSRC(x, D * 2);
    u32x4 xa = BLD128(rx, l * 24, 0, 0);
    u32x2 xb = __builtin_amdgcn_raw_buffer_load_b64(rx, l * 24 + 16, 0, 0);
    const unsigned e0 = __builtin_amdgcn_raw_buffer_load_b32(rws, WS_E, 0, AUX_SC1);
    SB();
    u32x4 W[18];
    auto r = RSRC(wq, D * NQ * 2);
    const int rq = s >> 2, cs = s & 3;
    const int base = ((rq * 144 + 4 * rho) * NQ + g * 256 + cs * 64 + c8 * 8) * 2;
#pragma unroll
    for (int u = 0; u < 16; ++u) W[u] = BLD128(r, base, (32 * (u >> 2) + (u & 3)) * NQ * 2, AUX_W);
    const int base2 = ((rq * 144 + 128 + 2 * rho) * NQ + g * 256 + cs * 64 + c8 * 8) * 2;
    W[16] = BLD128(r, base2, 0, AUX_W); W[17] = BLD128(r, base2, NQ * 2, AUX_W);
    const unsigned b1v = __builtin_amdgcn_raw_buffer_load_b16(RSRC(b1, F * 2), (18 * b + 2 * c16 + (h4 & 1)) * 2, 0, 0);
    if (l < 8) flag[l] = 0u;
    LDS_WAIT();
    __builtin_amdgcn_s_barrier();
    asm volatile("" : "+v"(xa), "+v"(xb) :: "memory");
    TS(1);
    const bool act = l < 48;
    float v[12]; unsigned d[12];
#pragma unroll
    for (int i = 0; i < 4; ++i) { v[2 * i] = lo16(xa[i]); v[2 * i + 1] = hi16(xa[i]); }
#pragma unroll
    for (int i = 0; i < 2; ++i) { v[8 + 2 * i] = lo16(xb[i]); v[9 + 2 * i] = hi16(xb[i]); }
    layernorm(v, d, act);
    if (act) {
#pragma unroll
      for (int i = 0; i < 3; ++i) *(LAS u32x4*)(lds + L_X + l * 48 + 16 * i) = u32x4{d[4 * i], d[4 * i + 1], d[4 * i + 2], d[4 * i + 3]};
    }
    LDS_WAIT();
    u32x4 av[4]; u32x2 av2;
#pragma unroll
    for (int i = 0; i < 4; ++i) av[i] = *(LAS u32x4*)(lds + L_X + (rq * 144 + 32 * i + 4 * rho) * 4);
    av2 = *(LAS u32x2*)(lds + L_X + (rq * 144 + 128 + 2 * rho) * 4);
    f32x4 acc0 = {0.f, 0.f, 0.f, 0.f}, acc1 = acc0;
#pragma unroll
    for (int u = 0; u < 16; u += 2) {
      acc0 = mfma(msk & av[u >> 2][u & 3], W[u], acc0);
      acc1 = mfma(msk & av[u >> 2][(u & 3) + 1], W[u + 1], acc1);
    }
    acc0 = mfma(msk & av2[0], W[16], acc0);
    acc1 = mfma(msk & av2[1], W[17], acc1);
    const f32x2 pr = fold_pair(acc0 + acc1);
    tag = __builtin_amdgcn_readfirstlane(e0) * 1664525u + 1013904223u;
    BST128((u32x4{asu(pr[0]), tag, asu(pr[1]), tag}), rws,
           (l & 8) ? OOB : WS_QG + ((g * 4 + rq) * 128 + cs * 32 + 4 * c8 + 2 * (h4 & 1) + (h4 >> 1)) * 16, AUX_SC1);
    CFENCE();
    TS(2);
    layernorm(v, d, act);
    if (act) {
#pragma unroll
      for (int i = 0; i < 3; ++i) *(LAS u32x4*)(lds + L_Y + l * 48 + 16 * i) = u32x4{d[4 * i], d[4 * i + 1], d[4 * i + 2], d[4 * i + 3]};
    }
    if (QDELAY) __builtin_amdgcn_s_sleep(QDELAY);
    {
      u32x4 G[8];
      _Pragma("nounroll") for (unsigned it = 0; it < SPIN_CAP; ++it) {
        CFENCE();
        bool ok = true;
#pragma unroll
        for (int k = 0; k < 8; ++k) G[k] = BLD128(rws, WS_QG + g * 8192 + k * 1024 + l * 16, 0, AUX_SC1);
#pragma unroll
        for (int k = 0; k < 8; ++k) ok &= TAGOK(G[k]);
        if (__all(ok)) break;
      }
#pragma unroll
      for (int k = 0; k < 2; ++k)
        *(LAS unsigned*)(lds + L_Q + (k * 64 + l) * 4) = cvtpk((asf(G[k][0]) + asf(G[k + 2][0])) + (asf(G[k + 4][0]) + asf(G[k + 6][0])),
                                                                (asf(G[k][2]) + asf(G[k + 2][2])) + (asf(G[k + 4][2]) + asf(G[k + 6][2])));
    }
    if (l == 0) flag[FL_TAG] = tag;
    LDS_WAIT();
    if (l == 0) flag[FL_Q] = 1u;
    TS(3);
    lds_wait_ge(flag + FL_OPUB, 1);
    {
      u32x4 O0, O1, M0, M1;
      const int og = WS_OG + (((g * 16 + s) * 16 + c8) * 8 + rho) * 16;
      const int ml = WS_ML + ((g * 2 + (s >> 3)) * 16 + c8) * 16;
      _Pragma("nounroll") for (unsigned it = 0; it < SPIN_CAP; ++it) {
        CFENCE();
        O0 = BLD128(rws, og, 0, AUX_SC1); O1 = BLD128(rws, og + 1024, 0, AUX_SC1);
        M0 = BLD128(rws, ml, 0, AUX_SC1); M1 = BLD128(rws, ml + 128, 0, AUX_SC1);
        if (__all(TAGOK(O0) & TAGOK(O1) & TAGOK(M0) & TAGOK(M1))) break;
      }
      const float m0 = asf(M0[0]), m1 = asf(M1[0]);
      const float M = max8(fmaxf(m0, m1));
      const float e0_ = __builtin_amdgcn_exp2f((m0 - M) * LOG2E), e1_ = __builtin_amdgcn_exp2f((m1 - M) * LOG2E);
      const float L = sum8(e0_ * asf(M0[2]) + e1_ * asf(M1[2]));
      const float n0 = sum8(e0_ * asf(O0[0]) + e1_ * asf(O1[0])), n1 = sum8(e0_ * asf(O0[2]) + e1_ * asf(O1[2]));
      const float il = __builtin_amdgcn_rcpf(L);
      if (c8 == 0) *(LAS u32x2*)(lds + L_AO + rho * 8) = u32x2{cvtpk(n0 * il, n0 * il), cvtpk(n1 * il, n1 * il)};
    }
    TS(4);
    {
      u32x4 H[5];
      const int hg = c16 < 9 ? WS_HG + (h4 * 2304 + 9 * b + c16) * 16 : OOB;
      const int hg4 = h4 < 2 ? hg : OOB;
      _Pragma("nounroll") for (unsigned it = 0; it < SPIN_CAP; ++it) {
        CFENCE();
        bool ok = true;
#pragma unroll
        for (int k = 0; k < 5; ++k) H[k] = BLD128(rws, k < 4 ? hg : hg4, k * 4 * 2304 * 16, AUX_SC1);
#pragma unroll
        for (int k = 0; k < 4; ++k) ok &= TAGOK(H[k]);
        ok &= TAGOK(H[4]) | (h4 >= 2);
        if (__all(ok | (c16 >= 9))) break;
      }
      const float s0 = xrow_sum(((asf(H[0][0]) + asf(H[1][0])) + (asf(H[2][0]) + asf(H[3][0]))) + asf(H[4][0]));
      const float s1 = xrow_sum(((asf(H[0][2]) + asf(H[1][2])) + (asf(H[2][2]) + asf(H[3][2]))) + asf(H[4][2]));
      const float a = rb(rb((h4 & 1) ? s1 : s0) + lo16(b1v));
      const float z = a + 0.044715f * a * a * a;
      const float hh = a * __builtin_amdgcn_rcpf(1.f + __builtin_amdgcn_exp2f(z * (-2.f * 0.7978845608028654f * LOG2E)));
      if (l < 32) *(LAS unsigned*)(lds + L_AH + (2 * c16 + h4) * 4) = cvtpk(hh, hh);
    }
    LDS_WAIT();
    if (l == 0) flag[FL_A] = 1u;
    TS(5);
    fp = b;
  }
  if (fp >= 0) {
    const unsigned xo = __builtin_amdgcn_raw_buffer_load_b32(RSRC(x, D * 2), fp * 4, 0, 0);
    const unsigned bo = __builtin_amdgcn_raw_buffer_load_b32(RSRC(b2, D * 2), fp * 4, 0, 0);
    lds_wait_ge(flag + FL_OUT, 3);
    TS(6);
    u32x4 P[4];
    _Pragma("nounroll") for (unsigned it = 0; it < SPIN_CAP; ++it) {
      CFENCE();
      bool ok = true;
#pragma unroll
      for (int k = 0; k < 4; ++k) P[k] = BLD128(rws, WS_FG + (l * 288 + fp) * 16, k * 64 * 288 * 16, AUX_SC1);
#pragma unroll
      for (int k = 0; k < 4; ++k) ok &= TAGOK(P[k]);
      if (__all(ok)) break;
    }
    const float s0 = wave_sum((asf(P[0][0]) + asf(P[1][0])) + (asf(P[2][0]) + asf(P[3][0])));
    const float s1 = wave_sum((asf(P[0][2]) + asf(P[1][2])) + (asf(P[2][2]) + asf(P[3][2])));
    if (l == 0) {
      ((unsigned*)out)[fp] = cvtpk(lo16(xo) + s0 + lo16(bo), hi16(xo) + s1 + hi16(bo));
      if (fp == 0) __builtin_amdgcn_raw_buffer_store_b32(tag, rws, WS_E, 0, AUX_SC1);
    }
    TS(7);
  }
  TS_FLUSH();
}

extern "C" void kernel_launch(void* const* d_in, const int* in_sizes, int n_in, void* d_out, int out_size, void* d_ws, size_t ws_size,
                              hipStream_t stream) {
  const u16* x = (const u16*)d_in[0];
  const u16* kc = (const u16*)d_in[1];
  const u16* vc = (const u16*)d_in[2];
  const u16* wq = (const u16*)d_in[3];
  const u16* wo = (const u16*)d_in[4];
  const u16* w1 = (const u16*)d_in[6];
  const u16* b1 = (const u16*)d_in[7];
  const u16* w2 = (const u16*)d_in[8];
  const u16* b2 = (const u16*)d_in[9];
  fused_k<<<256, NTHR, 0, stream>>>(x, wq, kc, vc, (unsigned char*)d_ws, w1, w2, wo, b1, b2, (u16*)d_out);
}
```

```cpp
#define DIAG_MODE 0
#define DIAG_W 0
#define DIAG_K 0
#include <hip/hip_runtime.h>
#include <hip/hip_bf16.h>
#include <stdint.h>

typedef unsigned short u16;
typedef unsigned long long u64;
typedef __attribute__((ext_vector_type(8))) __bf16 bf16x8;
typedef __attribute__((ext_vector_type(2))) __bf16 bf16x2;
typedef __attribute__((ext_vector_type(4))) float f32x4;
typedef __attribute__((ext_vector_type(2))) float f32x2;
typedef __attribute__((ext_vector_type(4))) unsigned u32x4;
typedef __attribute__((ext_vector_type(2))) unsigned u32x2;
#define LAS __attribute__((address_space(3)))

#define AUX_W 2
#define WIN_A 3
#define WIN_W 8
#define GATE_AT 15
#define QDELAY 0
#define AUX_SC1 16

constexpr int D = 576, NQ = 4096, SK = 16384, DH = 128, WIN = 4096, F = 4608;
constexpr int NWAVE = 12, NTHR = NWAVE * 64;
constexpr int R_W = 1  , R_ATT = 4  ;
constexpr int WS_E = 0;
constexpr int WS_QG = 4096;
constexpr int WS_HG = WS_QG + 16 * 4 * 128 * 16;
constexpr int WS_OG = WS_HG + 18 * 2304 * 16;
constexpr int WS_ML = WS_OG + 256 * 16 * 8 * 16;
constexpr int WS_FG = WS_ML + 16 * 2 * 16 * 16;
constexpr int WS_END = WS_FG + 256 * 288 * 16;
constexpr int WS_DBG = 8 << 20;
constexpr int OOB = 0x7ff00000;
constexpr unsigned SPIN_CAP = 1u << 20;
enum { FL_Q = 0, FL_ISS = 1, FL_ARR = 2, FL_OPUB = 3, FL_A = 4, FL_OUT = 5, FL_TAG = 6 };
constexpr int L_FLAG = 0;
constexpr int L_Q = 64;
constexpr int L_X = L_Q + 512;
constexpr int L_Y = L_X + 2304;
constexpr int L_ML = L_Y + 2304;
constexpr int L_AO = L_ML + 128;
constexpr int L_AH = L_AO + 64;
constexpr int L_PART = L_AH + 128;
constexpr int L_END = L_PART + 8 * 1024;

#define RSRC(p, n) __builtin_amdgcn_make_buffer_rsrc((void*)(p), 0, (n), 0x00020000)
#define BLD128(r, off, soff, aux) __builtin_amdgcn_raw_buffer_load_b128((r), (off), (soff), (aux))
#define BST128(v, r, off, aux) __builtin_amdgcn_raw_buffer_store_b128((v), (r), (off), 0, (aux))
#define CFENCE() asm volatile("" ::: "memory")
#define LDS_WAIT() asm volatile("s_waitcnt lgkmcnt(0)" ::: "memory")
#define KEEP(v) asm volatile("" :: "v"(v))
#define SB() __builtin_amdgcn_sched_barrier(0)
#define LDS_INC(i) __hip_atomic_fetch_add((LAS unsigned*)(flag + (i)), 1u, __ATOMIC_RELAXED, __HIP_MEMORY_SCOPE_WORKGROUP)
#define TAGOK(v) (((v)[1] == tag) & ((v)[3] == tag))
constexpr float LOG2E = 1.4426950408889634f;

#define STAMP
#define TS(i) do { u64 t_; SB(); asm volatile("s_memrealtime %0\n\ts_waitcnt lgkmcnt(0)" : "=s"(t_) :: "memory"); SB(); tsv[i] = t_; } while (0)
#define TS_FLUSH() do { \
    auto rd_ = RSRC(ws + WS_DBG, 256 * 16 * 8 * 8); \
    if (l == 0) { _Pragma("unroll") for (int i_ = 0; i_ < 8; ++i_) __builtin_amdgcn_raw_buffer_store_b64(u32x2{(unsigned)tsv[i_], tag}, rd_, ((b * 16 + w) * 8 + i_) * 8, 0, AUX_SC1); } \
    if (b == 0 && w == 0) { \
      u32x2 a_[4], z_[4]; \
      for (unsigned it = 0; it < SPIN_CAP; ++it) { CFENCE(); bool ok = true; \
        _Pragma("unroll") for (int k = 0; k < 4; ++k) { a_[k] = __builtin_amdgcn_raw_buffer_load_b64(rd_, (((k * 64 + l) * 16 + DIAG_W) * 8 + DIAG_K) * 8, 0, AUX_SC1); \
          z_[k] = __builtin_amdgcn_raw_buffer_load_b64(rd_, ((k * 64 + l) * 16 * 8) * 8, 0, AUX_SC1); ok &= (a_[k][1] == tag) & (z_[k][1] == tag); } \
        if (__all(ok)) break; } \
      const unsigned ref_ = (unsigned)tsv[0] - 1000u; float mn_ = 1e9f, mx_ = 0.f, sm_ = 0.f; \
      _Pragma("unroll") for (int k = 0; k < 4; ++k) { mn_ = fminf(mn_, (float)(z_[k][0] - ref_)); const float d_ = (float)(a_[k][0] - ref_); mx_ = fmaxf(mx_, d_); sm_ += d_; } \
      mn_ = -xrow_max(fmaxf(max8(-mn_), dpp<MIR>(max8(-mn_)))); mx_ = xrow_max(fmaxf(max8(mx_), dpp<MIR>(max8(mx_)))); sm_ = wave_sum(sm_) * (1.f / 256.f); \
      const float st_ = DIAG_MODE == 0 ? 0.f : DIAG_MODE == 1 ? mx_ - mn_ : sm_ - mn_; \
      const unsigned until_ = __builtin_amdgcn_readfirstlane((unsigned)(mn_ + 4000.f + st_)); \
      for (unsigned it = 0; it < SPIN_CAP; ++it) { u64 t_; asm volatile("s_memrealtime %0\n\ts_waitcnt lgkmcnt(0)" : "=s"(t_) :: "memory"); if ((unsigned)t_ - ref_ >= until_) break; } \
    } } while (0)

__device__ __forceinline__ float asf(unsigned u) { return __builtin_bit_cast(float, u); }
__device__ __forceinline__ unsigned asu(float f) { return __builtin_bit_cast(unsigned, f); }
__device__ __forceinline__ unsigned cvtpk(float lo, float hi) {
  bf16x2 r = {(__bf16)lo, (__bf16)hi};
  return __builtin_bit_cast(unsigned, r);
}
__device__ __forceinline__ float lo16(unsigned u) { return asf(u << 16); }
__device__ __forceinline__ float hi16(unsigned u) { return asf(u & 0xffff0000u); }
__device__ __forceinline__ float rb(float f) { return hi16(cvtpk(f, f)); }
template <int CTRL> __device__ __forceinline__ float dpp(float x) {
  return asf(__builtin_amdgcn_mov_dpp(asu(x), CTRL, 0xf, 0xf, true));
}
constexpr int XOR1 = 0xB1, XOR2 = 0x4E, HMIR = 0x141, MIR = 0x140, ROR8 = 0x128;
__device__ __forceinline__ float sum8(float x) { x += dpp<XOR1>(x); x += dpp<XOR2>(x); x += dpp<HMIR>(x); return x; }
__device__ __forceinline__ float max8(float x) { x = fmaxf(x, dpp<XOR1>(x)); x = fmaxf(x, dpp<XOR2>(x)); x = fmaxf(x, dpp<HMIR>(x)); return x; }
__device__ __forceinline__ float xrow_sum(float x) {
  auto s = __builtin_amdgcn_permlane16_swap(asu(x), asu(x), false, false);
  x = asf(s[0]) + asf(s[1]);
  auto t = __builtin_amdgcn_permlane32_swap(asu(x), asu(x), false, false);
  return asf(t[0]) + asf(t[1]);
}
__device__ __forceinline__ float xrow_max(float x) {
  auto s = __builtin_amdgcn_permlane16_swap(asu(x), asu(x), false, false);
  x = fmaxf(asf(s[0]), asf(s[1]));
  auto t = __builtin_amdgcn_permlane32_swap(asu(x), asu(x), false, false);
  return fmaxf(asf(t[0]), asf(t[1]));
}
__device__ __forceinline__ float wave_sum(float x) { x = sum8(x); x += dpp<MIR>(x); return xrow_sum(x); }
__device__ __forceinline__ f32x4 mfma(u32x4 a, u32x4 b, f32x4 c) {
  return __builtin_amdgcn_mfma_f32_16x16x32_bf16(__builtin_bit_cast(bf16x8, a), __builtin_bit_cast(bf16x8, b), c, 0, 0, 0);
}
__device__ __forceinline__ float dot8(u32x4 a, u32x4 b) {
  float d = 0.f;
#pragma unroll
  for (int k = 0; k < 4; ++k) { const unsigned ak = a[k], bk = b[k]; d = __builtin_amdgcn_fdot2_f32_bf16(__builtin_bit_cast(bf16x2, ak), __builtin_bit_cast(bf16x2, bk), d, false); }
  return d;
}
__device__ __forceinline__ f32x2 fold_pair(f32x4 acc) {
  unsigned u[4];
#pragma unroll
  for (int r = 0; r < 4; ++r) u[r] = __builtin_amdgcn_update_dpp(asu(acc[r]), asu(acc[r]), ROR8, 0xc, 0xf, false);
  auto s0 = __builtin_amdgcn_permlane32_swap(u[0], u[2], false, false);
  auto s1 = __builtin_amdgcn_permlane32_swap(u[1], u[3], false, false);
  return f32x2{asf(s0[0]) + asf(s0[1]), asf(s1[0]) + asf(s1[1])};
}
__device__ __forceinline__ void lds_wait_ge(volatile LAS unsigned* p, unsigned v) {
  _Pragma("nounroll") for (unsigned i = 0; *p < v && i < SPIN_CAP; ++i) __builtin_amdgcn_s_sleep(1);
  CFENCE();
}
__device__ __forceinline__ void layernorm(float (&v)[12], unsigned (&d)[12], bool act) {
  float s = 0.f;
#pragma unroll
  for (int i = 0; i < 12; i += 4) s += (v[i] + v[i + 1]) + (v[i + 2] + v[i + 3]);
  const float mean = wave_sum(s) * (1.f / D);
  float q0 = 0.f, q1 = 0.f;
#pragma unroll
  for (int i = 0; i < 12; i += 2) { const float a = v[i] - mean, c = v[i + 1] - mean; q0 = fmaf(a, a, q0); q1 = fmaf(c, c, q1); }
  const float var = wave_sum(act ? q0 + q1 : 0.f) * (1.f / D);
  const float mb = rb(mean);
  const float den = rb(__builtin_amdgcn_sqrtf(rb(rb(var) + 1e-5f)));
  const float inv = act ? __builtin_amdgcn_rcpf(den) : 0.f;
#pragma unroll
  for (int i = 0; i < 12; i += 2) {
    const unsigned t = cvtpk(v[i] - mb, v[i + 1] - mb);
    const float a = lo16(t) * inv, c = hi16(t) * inv;
    d[i] = cvtpk(a, a); d[i + 1] = cvtpk(c, c);
    v[i] = hi16(d[i]); v[i + 1] = hi16(d[i + 1]);
  }
}

__global__ void __launch_bounds__(NTHR) fused_k(const u16* x, const u16* wq, const u16* kc, const u16* vc, unsigned char* ws,
                                                const u16* w1, const u16* w2, const u16* wo, const u16* b1, const u16* b2, u16* out) {
  __shared__ __attribute__((aligned(16))) unsigned char lds_raw[L_END];
  LAS unsigned char* lds = (LAS unsigned char*)lds_raw;
  volatile LAS unsigned* flag = (volatile LAS unsigned*)(lds + L_FLAG);
  const int b = blockIdx.x, tid = threadIdx.x, w = __builtin_amdgcn_readfirstlane(tid >> 6), l = tid & 63;
  const int g = (b & 7) * 2 + (b >> 7), s = (b >> 3) & 15;
  const int rho = l >> 3, c8 = l & 7, h4 = l >> 4, c16 = l & 15;
  u64 tsv[8] = {0, 0, 0, 0, 0, 0, 0, 0};
  TS(0);
  auto rws = RSRC(ws, WS_END);
  u32x4 msk;
#pragma unroll
  for (int r = 0; r < 4; ++r) msk[r] = ((c8 >> 1) == r) ? ((c8 & 1) ? 0xffff0000u : 0x0000ffffu) : 0u;
  int fp = -1;
  unsigned tag = 0;

  if (w >= R_ATT) {
    const int aw = w - R_ATT;
    u32x4 KV[16];
    {
      const int kvb = (g * SK + (SK - WIN) + s * 256 + aw * 32) * DH * 2;
      const int rot = (((unsigned)(b * 8 + aw) * 0x9E3779B1u) >> 29) << 10;
      auto rk = RSRC(kc, 16 * SK * DH * 2);
      auto rv = RSRC(vc, 16 * SK * DH * 2);
#pragma unroll
      for (int i = 0; i < 16; ++i) {
        if (i >= WIN_A) KEEP(KV[i - WIN_A]);
        KV[i] = BLD128(i < 8 ? rk : rv, l * 16, kvb + (((i & 7) << 10) ^ rot), AUX_W);
        if (i == WIN_A - 1) { __builtin_amdgcn_s_barrier(); TS(1); }
        if (i == GATE_AT) { if (l == 0) LDS_INC(FL_ISS); }
        SB();
      }
    }
    CFENCE();
    TS(2);
    lds_wait_ge(flag + FL_Q, 1);
    const u32x4 qa = *(LAS u32x4*)(lds + L_Q + (c16 >> 3) * 256 + c16 * 16);
    const u32x4 qb = *(LAS u32x4*)(lds + L_Q + ((c16 >> 3) ^ 1) * 256 + c16 * 16);
    TS(3);
    float sc[8], m = -INFINITY;
#pragma unroll
    for (int i = 0; i < 8; ++i) {
      const float t = sum8(dot8(KV[i], qa) + dpp<ROR8>(dot8(KV[i], qb)));
      sc[i] = rb(rb(t) * 0.08838834764831845f);
      m = fmaxf(m, sc[i]);
    }
    m = xrow_max(m);
    float lsum = 0.f;
    unsigned pp[8];
#pragma unroll
    for (int i = 0; i < 8; ++i) { const float p = __builtin_amdgcn_exp2f((sc[i] - m) * LOG2E); lsum += p; pp[i] = cvtpk(p, p); }
    lsum = xrow_sum(lsum);
    if (l == 0 || l == 8) *(LAS f32x2*)(lds + L_ML + (rho * 8 + aw) * 8) = f32x2{m, lsum};
    TS(4);
    f32x4 o0 = {0.f, 0.f, 0.f, 0.f}, o1 = o0;
#pragma unroll
    for (int i = 0; i < 8; i += 2) { o0 = mfma(msk & pp[i], KV[8 + i], o0); o1 = mfma(msk & pp[i + 1], KV[9 + i], o1); }
    *(LAS f32x4*)(lds + L_PART + aw * 1024 + ((h4 >> 1) * 128 + 8 * c16 + 4 * (h4 & 1)) * 4) = o0 + o1;
    LDS_WAIT();
    TS(5);
    unsigned old = 0;
    if (l == 0) old = LDS_INC(FL_ARR);
    old = __builtin_amdgcn_readfirstlane(old);
    tag = __builtin_amdgcn_readfirstlane(flag[FL_TAG]);
    CFENCE();
    if (old == 7) {
      f32x4 ml[4], pa[8];
#pragma unroll
      for (int i = 0; i < 4; ++i) ml[i] = *(LAS f32x4*)(lds + L_ML + (l >> 5) * 64 + i * 16);
#pragma unroll
      for (int i = 0; i < 8; ++i) pa[i] = *(LAS f32x4*)(lds + L_PART + i * 1024 + l * 16);
      float M = -INFINITY;
#pragma unroll
      for (int i = 0; i < 4; ++i) M = fmaxf(M, fmaxf(ml[i][0], ml[i][2]));
      float L = 0.f; f32x4 o = {0.f, 0.f, 0.f, 0.f};
#pragma unroll
      for (int i = 0; i < 8; ++i) {
        const float e = __builtin_amdgcn_exp2f((ml[i >> 1][(i & 1) * 2] - M) * LOG2E);
        L = fmaf(e, ml[i >> 1][(i & 1) * 2 + 1], L);
        o += e * pa[i];
      }
      const int og = WS_OG + (((g * 16 + (l >> 2)) * 16 + s) * 8 + (l & 3)) * 16;
      BST128((u32x4{asu(o[0]), tag, asu(o[1]), tag}), rws, og, AUX_SC1);
      BST128((u32x4{asu(o[2]), tag, asu(o[3]), tag}), rws, og + 64, AUX_SC1);
      BST128((u32x4{asu(M), tag, asu(L), tag}), rws, (l & 31) == 0 ? WS_ML + ((g * 2 + (l >> 5)) * 16 + s) * 16 : OOB, AUX_SC1);
      CFENCE();
      if (l == 0) flag[FL_OPUB] = 1u;
      TS(6);
    }
    if (aw == 0 && b < 32) fp = 256 + b;
  } else if (w >= R_W) {
    const int ow = w - R_W;
    __builtin_amdgcn_s_barrier();
    CFENCE();
    const int p = b + 256 * ow;
    const int part = p / 36, slab = p - part * 36;
    auto r1 = RSRC(w1, p < 648 ? D * F * 2 : 0);
    auto r2 = RSRC(w2, F * D * 2);
    auto ro = RSRC(wo, NQ * D * 2);
    const int o1 = ((part * 32 + 8 * h4) * F + slab * 128 + c16 * 8) * 2;
    const int col = (ow * 192 + c8 * 8) * 2;
    const int o2 = (18 * b + 2 * rho) * D * 2 + col;
    const int o2c = rho < 2 ? (18 * b + 16 + rho) * D * 2 + col : OOB;
    const int oo = (g * 256 + 16 * s + 4 * (rho & 3) + 2 * (rho >> 2)) * D * 2 + col;
    lds_wait_ge(flag + FL_Q, 1);
    tag = __builtin_amdgcn_readfirstlane(flag[FL_TAG]);
    u32x4 av[2];
    av[0] = *(LAS u32x4*)(lds + L_Y + (part * 32 + 8 * h4) * 4);
    av[1] = *(LAS u32x4*)(lds + L_Y + (part * 32 + 8 * h4 + 4) * 4);
    lds_wait_ge(flag + FL_ISS, 8);
    TS(1);
    u32x4 W[23];
    f32x4 acc = {0.f, 0.f, 0.f, 0.f};
#pragma unroll
    for (int i = 0; i < 23 + WIN_W; ++i) {
      const int j = i - WIN_W;
      if (j >= 0 && j < 8) acc = mfma(msk & av[j >> 2][j & 3], W[j], acc);
      else if (j >= 0 && i < 23) KEEP(W[j]);
      if (i < 8) W[i] = BLD128(r1, o1, i * F * 2, AUX_W);
      else if (i < 17) { const int cb = (i - 8) / 3, u = (i - 8) % 3; W[i] = BLD128(r2, (u == 2 ? o2c : o2) + cb * 128 + (u == 1 ? D * 2 : 0), 0, AUX_W); }
      else if (i < 23) { const int cb = (i - 17) >> 1, u = (i - 17) & 1; W[i] = BLD128(ro, oo + cb * 128 + u * D * 2, 0, AUX_W); }
      if (j == 7) {
        const int pair = slab * 64 + 4 * c16 + 2 * (h4 & 1) + (h4 >> 1);
        const float v0 = l < 32 ? acc[0] : acc[2], v1 = l < 32 ? acc[1] : acc[3];
        BST128((u32x4{asu(v0), tag, asu(v1), tag}), rws, p < 648 ? WS_HG + (part * 2304 + pair) * 16 : OOB, AUX_SC1);
        TS(2);
      }
      SB();
    }
    CFENCE();
    TS(3);
    lds_wait_ge(flag + FL_A, 1);
    const u32x2 ah = *(LAS u32x2*)(lds + L_AH + rho * 8), ao = *(LAS u32x2*)(lds + L_AO + rho * 8);
    const unsigned ah2 = *(LAS unsigned*)(lds + L_AH + 64 + (rho & 1) * 4);
    TS(4);
    const u32x4 A0 = msk & ah[0], A1 = msk & ah[1], A2 = msk & ah2, A3 = msk & ao[0], A4 = msk & ao[1];
    f32x4 oc[3];
#pragma unroll
    for (int cb = 0; cb < 3; ++cb) oc[cb] = mfma(A0, W[8 + 3 * cb], f32x4{0.f, 0.f, 0.f, 0.f});
#pragma unroll
    for (int cb = 0; cb < 3; ++cb) oc[cb] = mfma(A1, W[9 + 3 * cb], oc[cb]);
#pragma unroll
    for (int cb = 0; cb < 3; ++cb) oc[cb] = mfma(A2, W[10 + 3 * cb], oc[cb]);
#pragma unroll
    for (int cb = 0; cb < 3; ++cb) oc[cb] = mfma(A3, W[17 + 2 * cb], oc[cb]);
#pragma unroll
    for (int cb = 0; cb < 3; ++cb) oc[cb] = mfma(A4, W[18 + 2 * cb], oc[cb]);
    const int fo = (l & 8) ? OOB : WS_FG + (b * 288 + ow * 96 + 4 * c8 + 2 * (h4 & 1) + (h4 >> 1)) * 16;
#pragma unroll
    for (int cb = 0; cb < 3; ++cb) {
      const f32x2 pr = fold_pair(oc[cb]);
      BST128((u32x4{asu(pr[0]), tag, asu(pr[1]), tag}), rws, fo + cb * 512, AUX_SC1);
    }
    CFENCE();
    if (l == 0) LDS_INC(FL_OUT);
    TS(5);
  } else {
    __builtin_amdgcn_s_setprio(3);
    auto rx = RSRC(x, D * 2);
    u32x4 xa = BLD128(rx, l * 24, 0, 0);
    u32x2 xb = __builtin_amdgcn_raw_buffer_load_b64(rx, l * 24 + 16, 0, 0);
    const unsigned e0 = __builtin_amdgcn_raw_buffer_load_b32(rws, WS_E, 0, AUX_SC1);
    SB();
    u32x4 W[18];
    auto r = RSRC(wq, D * NQ * 2);
    const int rq = s >> 2, cs = s & 3;
    const int base = ((rq * 144 + 4 * rho) * NQ + g * 256 + cs * 64 + c8 * 8) * 2;
#pragma unroll
    for (int u = 0; u < 16; ++u) W[u] = BLD128(r, base, (32 * (u >> 2) + (u & 3)) * NQ * 2, AUX_W);
    const int base2 = ((rq * 144 + 128 + 2 * rho) * NQ + g * 256 + cs * 64 + c8 * 8) * 2;
    W[16] = BLD128(r, base2, 0, AUX_W); W[17] = BLD128(r, base2, NQ * 2, AUX_W);
    const unsigned b1v = __builtin_amdgcn_raw_buffer_load_b16(RSRC(b1, F * 2), (18 * b + 2 * c16 + (h4 & 1)) * 2, 0, 0);
    if (l < 8) flag[l] = 0u;
    LDS_WAIT();
    __builtin_amdgcn_s_barrier();
    asm volatile("" : "+v"(xa), "+v"(xb) :: "memory");
    TS(1);
    const bool act = l < 48;
    float v[12]; unsigned d[12];
#pragma unroll
    for (int i = 0; i < 4; ++i) { v[2 * i] = lo16(xa[i]); v[2 * i + 1] = hi16(xa[i]); }
#pragma unroll
    for (int i = 0; i < 2; ++i) { v[8 + 2 * i] = lo16(xb[i]); v[9 + 2 * i] = hi16(xb[i]); }
    layernorm(v, d, act);
    if (act) {
#pragma unroll
      for (int i = 0; i < 3; ++i) *(LAS u32x4*)(lds + L_X + l * 48 + 16 * i) = u32x4{d[4 * i], d[4 * i + 1], d[4 * i + 2], d[4 * i + 3]};
    }
    LDS_WAIT();
    u32x4 av[4]; u32x2 av2;
#pragma unroll
    for (int i = 0; i < 4; ++i) av[i] = *(LAS u32x4*)(lds + L_X + (rq * 144 + 32 * i + 4 * rho) * 4);
    av2 = *(LAS u32x2*)(lds + L_X + (rq * 144 + 128 + 2 * rho) * 4);
    f32x4 acc0 = {0.f, 0.f, 0.f, 0.f}, acc1 = acc0;
#pragma unroll
    for (int u = 0; u < 16; u += 2) {
      acc0 = mfma(msk & av[u >> 2][u & 3], W[u], acc0);
      acc1 = mfma(msk & av[u >> 2][(u & 3) + 1], W[u + 1], acc1);
    }
    acc0 = mfma(msk & av2[0], W[16], acc0);
    acc1 = mfma(msk & av2[1], W[17], acc1);
    const f32x2 pr = fold_pair(acc0 + acc1);
    tag = __builtin_amdgcn_readfirstlane(e0) * 1664525u + 1013904223u;
    BST128((u32x4{asu(pr[0]), tag, asu(pr[1]), tag}), rws,
           (l & 8) ? OOB : WS_QG + ((g * 4 + rq) * 128 + cs * 32 + 4 * c8 + 2 * (h4 & 1) + (h4 >> 1)) * 16, AUX_SC1);
    CFENCE();
    TS(2);
    layernorm(v, d, act);
    if (act) {
#pragma unroll
      for (int i = 0; i < 3; ++i) *(LAS u32x4*)(lds + L_Y + l * 48 + 16 * i) = u32x4{d[4 * i], d[4 * i + 1], d[4 * i + 2], d[4 * i + 3]};
    }
    if (QDELAY) __builtin_amdgcn_s_sleep(QDELAY);
    {
      u32x4 G[8];
      _Pragma("nounroll") for (unsigned it = 0; it < SPIN_CAP; ++it) {
        CFENCE();
        bool ok = true;
#pragma unroll
        for (int k = 0; k < 8; ++k) G[k] = BLD128(rws, WS_QG + g * 8192 + k * 1024 + l * 16, 0, AUX_SC1);
#pragma unroll
        for (int k = 0; k < 8; ++k) ok &= TAGOK(G[k]);
        if (__all(ok)) break;
      }
#pragma unroll
      for (int k = 0; k < 2; ++k)
        *(LAS unsigned*)(lds + L_Q + (k * 64 + l) * 4) = cvtpk((asf(G[k][0]) + asf(G[k + 2][0])) + (asf(G[k + 4][0]) + asf(G[k + 6][0])),
                                                                (asf(G[k][2]) + asf(G[k + 2][2])) + (asf(G[k + 4][2]) + asf(G[k + 6][2])));
    }
    if (l == 0) flag[FL_TAG] = tag;
    LDS_WAIT();
    if (l == 0) flag[FL_Q] = 1u;
    TS(3);
    lds_wait_ge(flag + FL_OPUB, 1);
    {
      u32x4 O0, O1, M0, M1;
      const int og = WS_OG + (((g * 16 + s) * 16 + c8) * 8 + rho) * 16;
      const int ml = WS_ML + ((g * 2 + (s >> 3)) * 16 + c8) * 16;
      _Pragma("nounroll") for (unsigned it = 0; it < SPIN_CAP; ++it) {
        CFENCE();
        O0 = BLD128(rws, og, 0, AUX_SC1); O1 = BLD128(rws, og + 1024, 0, AUX_SC1);
        M0 = BLD128(rws, ml, 0, AUX_SC1); M1 = BLD128(rws, ml + 128, 0, AUX_SC1);
        if (__all(TAGOK(O0) & TAGOK(O1) & TAGOK(M0) & TAGOK(M1))) break;
      }
      const float m0 = asf(M0[0]), m1 = asf(M1[0]);
      const float M = max8(fmaxf(m0, m1));
      const float e0_ = __builtin_amdgcn_exp2f((m0 - M) * LOG2E), e1_ = __builtin_amdgcn_exp2f((m1 - M) * LOG2E);
      const float L = sum8(e0_ * asf(M0[2]) + e1_ * asf(M1[2]));
      const float n0 = sum8(e0_ * asf(O0[0]) + e1_ * asf(O1[0])), n1 = sum8(e0_ * asf(O0[2]) + e1_ * asf(O1[2]));
      const float il = __builtin_amdgcn_rcpf(L);
      if (c8 == 0) *(LAS u32x2*)(lds + L_AO + rho * 8) = u32x2{cvtpk(n0 * il, n0 * il), cvtpk(n1 * il, n1 * il)};
    }
    TS(4);
    {
      u32x4 H[5];
      const int hg = c16 < 9 ? WS_HG + (h4 * 2304 + 9 * b + c16) * 16 : OOB;
      const int hg4 = h4 < 2 ? hg : OOB;
      _Pragma("nounroll") for (unsigned it = 0; it < SPIN_CAP; ++it) {
        CFENCE();
        bool ok = true;
#pragma unroll
        for (int k = 0; k < 5; ++k) H[k] = BLD128(rws, k < 4 ? hg : hg4, k * 4 * 2304 * 16, AUX_SC1);
#pragma unroll
        for (int k = 0; k < 4; ++k) ok &= TAGOK(H[k]);
        ok &= TAGOK(H[4]) | (h4 >= 2);
        if (__all(ok | (c16 >= 9))) break;
      }
      const float s0 = xrow_sum(((asf(H[0][0]) + asf(H[1][0])) + (asf(H[2][0]) + asf(H[3][0]))) + asf(H[4][0]));
      const float s1 = xrow_sum(((asf(H[0][2]) + asf(H[1][2])) + (asf(H[2][2]) + asf(H[3][2]))) + asf(H[4][2]));
      const float a = rb(rb((h4 & 1) ? s1 : s0) + lo16(b1v));
      const float z = a + 0.044715f * a * a * a;
      const float hh = a * __builtin_amdgcn_rcpf(1.f + __builtin_amdgcn_exp2f(z * (-2.f * 0.7978845608028654f * LOG2E)));
      if (l < 32) *(LAS unsigned*)(lds + L_AH + (2 * c16 + h4) * 4) = cvtpk(hh, hh);
    }
    LDS_WAIT();
    if (l == 0) flag[FL_A] = 1u;
    TS(5);
    fp = b;
  }
  if (fp >= 0) {
    const unsigned xo = __builtin_amdgcn_raw_buffer_load_b32(RSRC(x, D * 2), fp * 4, 0, 0);
    const unsigned bo = __builtin_amdgcn_raw_buffer_load_b32(RSRC(b2, D * 2), fp * 4, 0, 0);
    lds_wait_ge(flag + FL_OUT, 3);
    TS(6);
    u32x4 P[4];
    _Pragma("nounroll") for (unsigned it = 0; it < SPIN_CAP; ++it) {
      CFENCE();
      bool ok = true;
#pragma unroll
      for (int k = 0; k < 4; ++k) P[k] = BLD128(rws, WS_FG + (l * 288 + fp) * 16, k * 64 * 288 * 16, AUX_SC1);
#pragma unroll
      for (int k = 0; k < 4; ++k) ok &= TAGOK(P[k]);
      if (__all(ok)) break;
    }
    const float s0 = wave_sum((asf(P[0][0]) + asf(P[1][0])) + (asf(P[2][0]) + asf(P[3][0])));
    const float s1 = wave_sum((asf(P[0][2]) + asf(P[1][2])) + (asf(P[2][2]) + asf(P[3][2])));
    if (l == 0) {
      ((unsigned*)out)[fp] = cvtpk(lo16(xo) + s0 + lo16(bo), hi16(xo) + s1 + hi16(bo));
      if (fp == 0) __builtin_amdgcn_raw_buffer_store_b32(tag, rws, WS_E, 0, AUX_SC1);
    }
    TS(7);
  }
  TS_FLUSH();
}

extern "C" void kernel_launch(void* const* d_in, const int* in_sizes, int n_in, void* d_out, int out_size, void* d_ws, size_t ws_size,
                              hipStream_t stream) {
  const u16* x = (const u16*)d_in[0];
  const u16* kc = (const u16*)d_in[1];
  const u16* vc = (const u16*)d_in[2];
  const u16* wq = (const u16*)d_in[3];
  const u16* wo = (const u16*)d_in[4];
  const u16* w1 = (const u16*)d_in[6];
  const u16* b1 = (const u16*)d_in[7];
  const u16* w2 = (const u16*)d_in[8];
  const u16* b2 = (const u16*)d_in[9];
  fused_k<<<256, NTHR, 0, stream>>>(x, wq, kc, vc, (unsigned char*)d_ws, w1, w2, wo, b1, b2, (u16*)d_out);
}
```

```cpp
#define DIAG_MODE 0
#define DIAG_W 0
#define DIAG_K 0
#define XV 1
#include <hip/hip_runtime.h>
#include <hip/hip_bf16.h>
#include <stdint.h>

typedef unsigned short u16;
typedef unsigned long long u64;
typedef __attribute__((ext_vector_type(8))) __bf16 bf16x8;
typedef __attribute__((ext_vector_type(2))) __bf16 bf16x2;
typedef __attribute__((ext_vector_type(4))) float f32x4;
typedef __attribute__((ext_vector_type(2))) float f32x2;
typedef __attribute__((ext_vector_type(4))) unsigned u32x4;
typedef __attribute__((ext_vector_type(2))) unsigned u32x2;
#define LAS __attribute__((address_space(3)))

#define AUX_W 2
#define WIN_A 3
#define WIN_W 8
#define GATE_AT 15
#define QDELAY 0
#define AUX_SC1 16

constexpr int D = 576, NQ = 4096, SK = 16384, DH = 128, WIN = 4096, F = 4608;
constexpr int NWAVE = 12, NTHR = NWAVE * 64;
constexpr int R_W = 1  , R_ATT = 4  ;
constexpr int WS_E = 0;
constexpr int WS_QG = 4096;
constexpr int WS_HG = WS_QG + 16 * 4 * 128 * 16;
constexpr int WS_OG = WS_HG + 18 * 2304 * 16;
constexpr int WS_ML = WS_OG + 256 * 16 * 8 * 16;
constexpr int WS_FG = WS_ML + 16 * 2 * 16 * 16;
constexpr int WS_END = WS_FG + 256 * 288 * 16;
constexpr int WS_DBG = 8 << 20;
constexpr int OOB = 0x7ff00000;
constexpr unsigned SPIN_CAP = 1u << 20;
enum { FL_Q = 0, FL_ISS = 1, FL_ARR = 2, FL_OPUB = 3, FL_A = 4, FL_OUT = 5, FL_TAG = 6 };
constexpr int L_FLAG = 0;
constexpr int L_Q = 64;
constexpr int L_X = L_Q + 512;
constexpr int L_Y = L_X + 2304;
constexpr int L_ML = L_Y + 2304;
constexpr int L_AO = L_ML + 128;
constexpr int L_AH = L_AO + 64;
constexpr int L_PART = L_AH + 128;
constexpr int L_END = L_PART + 8 * 1024;

#define RSRC(p, n) __builtin_amdgcn_make_buffer_rsrc((void*)(p), 0, (n), 0x00020000)
#define BLD128(r, off, soff, aux) __builtin_amdgcn_raw_buffer_load_b128((r), (off), (soff), (aux))
#define BST128(v, r, off, aux) __builtin_amdgcn_raw_buffer_store_b128((v), (r), (off), 0, (aux))
#define CFENCE() asm volatile("" ::: "memory")
#define LDS_WAIT() asm volatile("s_waitcnt lgkmcnt(0)" ::: "memory")
#define KEEP(v) asm volatile("" :: "v"(v))
#define SB() __builtin_amdgcn_sched_barrier(0)
#define LDS_INC(i) __hip_atomic_fetch_add((LAS unsigned*)(flag + (i)), 1u, __ATOMIC_RELAXED, __HIP_MEMORY_SCOPE_WORKGROUP)
#define TAGOK(v) (((v)[1] == tag) & ((v)[3] == tag))
constexpr float LOG2E = 1.4426950408889634f;

#define STAMP
#define TS(i) do { u64 t_; SB(); asm volatile("s_memrealtime %0\n\ts_waitcnt lgkmcnt(0)" : "=s"(t_) :: "memory"); SB(); tsv[i] = t_; } while (0)
#define TS_FLUSH() do { \
    auto rd_ = RSRC(ws + WS_DBG, 256 * 16 * 8 * 8); \
    if (l == 0) { _Pragma("unroll") for (int i_ = 0; i_ < 8; ++i_) __builtin_amdgcn_raw_buffer_store_b64(u32x2{(unsigned)tsv[i_], tag}, rd_, ((b * 16 + w) * 8 + i_) * 8, 0, AUX_SC1); } \
    if (b == 0 && w == 0) { \
      u32x2 a_[4], z_[4]; \
      for (unsigned it = 0; it < SPIN_CAP; ++it) { CFENCE(); bool ok = true; \
        _Pragma("unroll") for (int k = 0; k < 4; ++k) { a_[k] = __builtin_amdgcn_raw_buffer_load_b64(rd_, (((k * 64 + l) * 16 + DIAG_W) * 8 + DIAG_K) * 8, 0, AUX_SC1); \
          z_[k] = __builtin_amdgcn_raw_buffer_load_b64(rd_, ((k * 64 + l) * 16 * 8) * 8, 0, AUX_SC1); ok &= (a_[k][1] == tag) & (z_[k][1] == tag); } \
        if (__all(ok)) break; } \
      const unsigned ref_ = (unsigned)tsv[0] - 3000u; float mn_ = 1e9f, mx_ = 0.f, sm_ = 0.f; \
      _Pragma("unroll") for (int k = 0; k < 4; ++k) { mn_ = fminf(mn_, (float)(z_[k][0] - ref_)); const float d_ = (float)(a_[k][0] - ref_); mx_ = fmaxf(mx_, d_); sm_ += d_; } \
      mn_ = -xrow_max(fmaxf(max8(-mn_), dpp<MIR>(max8(-mn_)))); mx_ = xrow_max(fmaxf(max8(mx_), dpp<MIR>(max8(mx_)))); sm_ = wave_sum(sm_) * (1.f / 256.f); \
      const float st_ = DIAG_MODE == 0 ? 0.f : DIAG_MODE == 1 ? mx_ - mn_ : sm_ - mn_; \
      const unsigned ta_ = *(volatile unsigned*)(ws + WS_DBG - 64) - ref_; \
      if (XV == 2) { if (l == 0) *(volatile unsigned*)(ws + WS_DBG - 64) = ref_ + (unsigned)mn_; } \
      const unsigned until_ = (XV == 2 || XV == 3) ? 0u : __builtin_amdgcn_readfirstlane(XV == 4 ? (unsigned)mn_ + 4000u + ((unsigned)mn_ - ta_) - ((unsigned)mn_ - ta_) : ta_ + 4000u); \
      for (unsigned it = 0; it < SPIN_CAP; ++it) { u64 t_; asm volatile("s_memrealtime %0\n\ts_waitcnt lgkmcnt(0)" : "=s"(t_) :: "memory"); if ((unsigned)t_ - ref_ >= until_) break; } \
    } } while (0)

__device__ __forceinline__ float asf(unsigned u) { return __builtin_bit_cast(float, u); }
__device__ __forceinline__ unsigned asu(float f) { return __builtin_bit_cast(unsigned, f); }
__device__ __forceinline__ unsigned cvtpk(float lo, float hi) {
  bf16x2 r = {(__bf16)lo, (__bf16)hi};
  return __builtin_bit_cast(unsigned, r);
}
__device__ __forceinline__ float lo16(unsigned u) { return asf(u << 16); }
__device__ __forceinline__ float hi16(unsigned u) { return asf(u & 0xffff0000u); }
__device__ __forceinline__ float rb(float f) { return hi16(cvtpk(f, f)); }
template <int CTRL> __device__ __forceinline__ float dpp(float x) {
  return asf(__builtin_amdgcn_mov_dpp(asu(x), CTRL, 0xf, 0xf, true));
}
constexpr int XOR1 = 0xB1, XOR2 = 0x4E, HMIR = 0x141, MIR = 0x140, ROR8 = 0x128;
__device__ __forceinline__ float sum8(float x) { x += dpp<XOR1>(x); x += dpp<XOR2>(x); x += dpp<HMIR>(x); return x; }
__device__ __forceinline__ float max8(float x) { x = fmaxf(x, dpp<XOR1>(x)); x = fmaxf(x, dpp<XOR2>(x)); x = fmaxf(x, dpp<HMIR>(x)); return x; }
__device__ __forceinline__ float xrow_sum(float x) {
  auto s = __builtin_amdgcn_permlane16_swap(asu(x), asu(x), false, false);
  x = asf(s[0]) + asf(s[1]);
  auto t = __builtin_amdgcn_permlane32_swap(asu(x), asu(x), false, false);
  return asf(t[0]) + asf(t[1]);
}
__device__ __forceinline__ float xrow_max(float x) {
  auto s = __builtin_amdgcn_permlane16_swap(asu(x), asu(x), false, false);
  x = fmaxf(asf(s[0]), asf(s[1]));
  auto t = __builtin_amdgcn_permlane32_swap(asu(x), asu(x), false, false);
  return fmaxf(asf(t[0]), asf(t[1]));
}
__device__ __forceinline__ float wave_sum(float x) { x = sum8(x); x += dpp<MIR>(x); return xrow_sum(x); }
__device__ __forceinline__ f32x4 mfma(u32x4 a, u32x4 b, f32x4 c) {
  return __builtin_amdgcn_mfma_f32_16x16x32_bf16(__builtin_bit_cast(bf16x8, a), __builtin_bit_cast(bf16x8, b), c, 0, 0, 0);
}
__device__ __forceinline__ float dot8(u32x4 a, u32x4 b) {
  float d = 0.f;
#pragma unroll
  for (int k = 0; k < 4; ++k) { const unsigned ak = a[k], bk = b[k]; d = __builtin_amdgcn_fdot2_f32_bf16(__builtin_bit_cast(bf16x2, ak), __builtin_bit_cast(bf16x2, bk), d, false); }
  return d;
}
__device__ __forceinline__ f32x2 fold_pair(f32x4 acc) {
  unsigned u[4];
#pragma unroll
  for (int r = 0; r < 4; ++r) u[r] = __builtin_amdgcn_update_dpp(asu(acc[r]), asu(acc[r]), ROR8, 0xc, 0xf, false);
  auto s0 = __builtin_amdgcn_permlane32_swap(u[0], u[2], false, false);
  auto s1 = __builtin_amdgcn_permlane32_swap(u[1], u[3], false, false);
  return f32x2{asf(s0[0]) + asf(s0[1]), asf(s1[0]) + asf(s1[1])};
}
__device__ __forceinline__ void lds_wait_ge(volatile LAS unsigned* p, unsigned v) {
  _Pragma("nounroll") for (unsigned i = 0; *p < v && i < SPIN_CAP; ++i) __builtin_amdgcn_s_sleep(1);
  CFENCE();
}
__device__ __forceinline__ void layernorm(float (&v)[12], unsigned (&d)[12], bool act) {
  float s = 0.f;
#pragma unroll
  for (int i = 0; i < 12; i += 4) s += (v[i] + v[i + 1]) + (v[i + 2] + v[i + 3]);
  const float mean = wave_sum(s) * (1.f / D);
  float q0 = 0.f, q1 = 0.f;
#pragma unroll
  for (int i = 0; i < 12; i += 2) { const float a = v[i] - mean, c = v[i + 1] - mean; q0 = fmaf(a, a, q0); q1 = fmaf(c, c, q1); }
  const float var = wave_sum(act ? q0 + q1 : 0.f) * (1.f / D);
  const float mb = rb(mean);
  const float den = rb(__builtin_amdgcn_sqrtf(rb(rb(var) + 1e-5f)));
  const float inv = act ? __builtin_amdgcn_rcpf(den) : 0.f;
#pragma unroll
  for (int i = 0; i < 12; i += 2) {
    const unsigned t = cvtpk(v[i] - mb, v[i + 1] - mb);
    const float a = lo16(t) * inv, c = hi16(t) * inv;
    d[i] = cvtpk(a, a); d[i + 1] = cvtpk(c, c);
    v[i] = hi16(d[i]); v[i + 1] = hi16(d[i + 1]);
  }
}

__global__ void __launch_bounds__(NTHR) fused_k(const u16* x, const u16* wq, const u16* kc, const u16* vc, unsigned char* ws,
                                                const u16* w1, const u16* w2, const u16* wo, const u16* b1, const u16* b2, u16* out) {
  __shared__ __attribute__((aligned(16))) unsigned char lds_raw[L_END];
  LAS unsigned char* lds = (LAS unsigned char*)lds_raw;
  volatile LAS unsigned* flag = (volatile LAS unsigned*)(lds + L_FLAG);
  const int b = blockIdx.x, tid = threadIdx.x, w = __builtin_amdgcn_readfirstlane(tid >> 6), l = tid & 63;
  const int g = (b & 7) * 2 + (b >> 7), s = (b >> 3) & 15;
  const int rho = l >> 3, c8 = l & 7, h4 = l >> 4, c16 = l & 15;
  u64 tsv[8] = {0, 0, 0, 0, 0, 0, 0, 0};
  TS(0);
  auto rws = RSRC(ws, WS_END);
  u32x4 msk;
#pragma unroll
  for (int r = 0; r < 4; ++r) msk[r] = ((c8 >> 1) == r) ? ((c8 & 1) ? 0xffff0000u : 0x0000ffffu) : 0u;
  int fp = -1;
  unsigned tag = 0;

  if (w >= R_ATT) {
    const int aw = w - R_ATT;
    u32x4 KV[16];
    {
      const int kvb = (g * SK + (SK - WIN) + s * 256 + aw * 32) * DH * 2;
      const int rot = (((unsigned)(b * 8 + aw) * 0x9E3779B1u) >> 29) << 10;
      auto rk = RSRC(kc, 16 * SK * DH * 2);
      auto rv = RSRC(vc, 16 * SK * DH * 2);
#pragma unroll
      for (int i = 0; i < 16; ++i) {
        if (i >= WIN_A) KEEP(KV[i - WIN_A]);
        KV[i] = BLD128(i < 8 ? rk : rv, l * 16, kvb + (((i & 7) << 10) ^ rot), AUX_W);
        if (i == WIN_A - 1) { __builtin_amdgcn_s_barrier(); TS(1); }
        if (i == GATE_AT) { if (l == 0) LDS_INC(FL_ISS); }
        SB();
      }
    }
    CFENCE();
    TS(2);
    lds_wait_ge(flag + FL_Q, 1);
    const u32x4 qa = *(LAS u32x4*)(lds + L_Q + (c16 >> 3) * 256 + c16 * 16);
    const u32x4 qb = *(LAS u32x4*)(lds + L_Q + ((c16 >> 3) ^ 1) * 256 + c16 * 16);
    TS(3);
    float sc[8], m = -INFINITY;
#pragma unroll
    for (int i = 0; i < 8; ++i) {
      const float t = sum8(dot8(KV[i], qa) + dpp<ROR8>(dot8(KV[i], qb)));
      sc[i] = rb(rb(t) * 0.08838834764831845f);
      m = fmaxf(m, sc[i]);
    }
    m = xrow_max(m);
    float lsum = 0.f;
    unsigned pp[8];
#pragma unroll
    for (int i = 0; i < 8; ++i) { const float p = __builtin_amdgcn_exp2f((sc[i] - m) * LOG2E); lsum += p; pp[i] = cvtpk(p, p); }
    lsum = xrow_sum(lsum);
    if (l == 0 || l == 8) *(LAS f32x2*)(lds + L_ML + (rho * 8 + aw) * 8) = f32x2{m, lsum};
    TS(4);
    f32x4 o0 = {0.f, 0.f, 0.f, 0.f}, o1 = o0;
#pragma unroll
    for (int i = 0; i < 8; i += 2) { o0 = mfma(msk & pp[i], KV[8 + i], o0); o1 = mfma(msk & pp[i + 1], KV[9 + i], o1); }
    *(LAS f32x4*)(lds + L_PART + aw * 1024 + ((h4 >> 1) * 128 + 8 * c16 + 4 * (h4 & 1)) * 4) = o0 + o1;
    LDS_WAIT();
    TS(5);
    unsigned old = 0;
    if (l == 0) old = LDS_INC(FL_ARR);
    old = __builtin_amdgcn_readfirstlane(old);
    tag = __builtin_amdgcn_readfirstlane(flag[FL_TAG]);
    CFENCE();
    if (old == 7) {
      f32x4 ml[4], pa[8];
#pragma unroll
      for (int i = 0; i < 4; ++i) ml[i] = *(LAS f32x4*)(lds + L_ML + (l >> 5) * 64 + i * 16);
#pragma unroll
      for (int i = 0; i < 8; ++i) pa[i] = *(LAS f32x4*)(lds + L_PART + i * 1024 + l * 16);
      float M = -INFINITY;
#pragma unroll
      for (int i = 0; i < 4; ++i) M = fmaxf(M, fmaxf(ml[i][0], ml[i][2]));
      float L = 0.f; f32x4 o = {0.f, 0.f, 0.f, 0.f};
#pragma unroll
      for (int i = 0; i < 8; ++i) {
        const float e = __builtin_amdgcn_exp2f((ml[i >> 1][(i & 1) * 2] - M) * LOG2E);
        L = fmaf(e, ml[i >> 1][(i & 1) * 2 + 1], L);
        o += e * pa[i];
      }
      const int og = WS_OG + (((g * 16 + (l >> 2)) * 16 + s) * 8 + (l & 3)) * 16;
      BST128((u32x4{asu(o[0]), tag, asu(o[1]), tag}), rws, og, AUX_SC1);
      BST128((u32x4{asu(o[2]), tag, asu(o[3]), tag}), rws, og + 64, AUX_SC1);
      BST128((u32x4{asu(M), tag, asu(L), tag}), rws, (l & 31) == 0 ? WS_ML + ((g * 2 + (l >> 5)) * 16 + s) * 16 : OOB, AUX_SC1);
      CFENCE();
      if (l == 0) flag[FL_OPUB] = 1u;
      TS(6);
    }
    if (aw == 0 && b < 32) fp = 256 + b;
  } else if (w >= R_W) {
    const int ow = w - R_W;
    __builtin_amdgcn_s_barrier();
    CFENCE();
    const int p = b + 256 * ow;
    const int part = p / 36, slab = p - part * 36;
    auto r1 = RSRC(w1, p < 648 ? D * F * 2 : 0);
    auto r2 = RSRC(w2, F * D * 2);
    auto ro = RSRC(wo, NQ * D * 2);
    const int o1 = ((part * 32 + 8 * h4) * F + slab * 128 + c16 * 8) * 2;
    const int col = (ow * 192 + c8 * 8) * 2;
    const int o2 = (18 * b + 2 * rho) * D * 2 + col;
    const int o2c = rho < 2 ? (18 * b + 16 + rho) * D * 2 + col : OOB;
    const int oo = (g * 256 + 16 * s + 4 * (rho & 3) + 2 * (rho >> 2)) * D * 2 + col;
    lds_wait_ge(flag + FL_Q, 1);
    tag = __builtin_amdgcn_readfirstlane(flag[FL_TAG]);
    u32x4 av[2];
    av[0] = *(LAS u32x4*)(lds + L_Y + (part * 32 + 8 * h4) * 4);
    av[1] = *(LAS u32x4*)(lds + L_Y + (part * 32 + 8 * h4 + 4) * 4);
    lds_wait_ge(flag + FL_ISS, 8);
    TS(1);
    u32x4 W[23];
    f32x4 acc = {0.f, 0.f, 0.f, 0.f};
#pragma unroll
    for (int i = 0; i < 23 + WIN_W; ++i) {
      const int j = i - WIN_W;
      if (j >= 0 && j < 8) acc = mfma(msk & av[j >> 2][j & 3], W[j], acc);
      else if (j >= 0 && i < 23) KEEP(W[j]);
      if (i < 8) W[i] = BLD128(r1, o1, i * F * 2, AUX_W);
      else if (i < 17) { const int cb = (i - 8) / 3, u = (i - 8) % 3; W[i] = BLD128(r2, (u == 2 ? o2c : o2) + cb * 128 + (u == 1 ? D * 2 : 0), 0, AUX_W); }
      else if (i < 23) { const int cb = (i - 17) >> 1, u = (i - 17) & 1; W[i] = BLD128(ro, oo + cb * 128 + u * D * 2, 0, AUX_W); }
      if (j == 7) {
        const int pair = slab * 64 + 4 * c16 + 2 * (h4 & 1) + (h4 >> 1);
        const float v0 = l < 32 ? acc[0] : acc[2], v1 = l < 32 ? acc[1] : acc[3];
        BST128((u32x4{asu(v0), tag, asu(v1), tag}), rws, p < 648 ? WS_HG + (part * 2304 + pair) * 16 : OOB, AUX_SC1);
        TS(2);
      }
      SB();
    }
    CFENCE();
    TS(3);
    lds_wait_ge(flag + FL_A, 1);
    const u32x2 ah = *(LAS u32x2*)(lds + L_AH + rho * 8), ao = *(LAS u32x2*)(lds + L_AO + rho * 8);
    const unsigned ah2 = *(LAS unsigned*)(lds + L_AH + 64 + (rho & 1) * 4);
    TS(4);
    const u32x4 A0 = msk & ah[0], A1 = msk & ah[1], A2 = msk & ah2, A3 = msk & ao[0], A4 = msk & ao[1];
    f32x4 oc[3];
#pragma unroll
    for (int cb = 0; cb < 3; ++cb) oc[cb] = mfma(A0, W[8 + 3 * cb], f32x4{0.f, 0.f, 0.f, 0.f});
#pragma unroll
    for (int cb = 0; cb < 3; ++cb) oc[cb] = mfma(A1, W[9 + 3 * cb], oc[cb]);
#pragma unroll
    for (int cb = 0; cb < 3; ++cb) oc[cb] = mfma(A2, W[10 + 3 * cb], oc[cb]);
#pragma unroll
    for (int cb = 0; cb < 3; ++cb) oc[cb] = mfma(A3, W[17 + 2 * cb], oc[cb]);
#pragma unroll
    for (int cb = 0; cb < 3; ++cb) oc[cb] = mfma(A4, W[18 + 2 * cb], oc[cb]);
    const int fo = (l & 8) ? OOB : WS_FG + (b * 288 + ow * 96 + 4 * c8 + 2 * (h4 & 1) + (h4 >> 1)) * 16;
#pragma unroll
    for (int cb = 0; cb < 3; ++cb) {
      const f32x2 pr = fold_pair(oc[cb]);
      BST128((u32x4{asu(pr[0]), tag, asu(pr[1]), tag}), rws, fo + cb * 512, AUX_SC1);
    }
    CFENCE();
    if (l == 0) LDS_INC(FL_OUT);
    TS(5);
  } else {
    __builtin_amdgcn_s_setprio(3);
    auto rx = RSRC(x, D * 2);
    u32x4 xa = BLD128(rx, l * 24, 0, 0);
    u32x2 xb = __builtin_amdgcn_raw_buffer_load_b64(rx, l * 24 + 16, 0, 0);
    const unsigned e0 = __builtin_amdgcn_raw_buffer_load_b32(rws, WS_E, 0, AUX_SC1);
    SB();
    u32x4 W[18];
    auto r = RSRC(wq, D * NQ * 2);
    const int rq = s >> 2, cs = s & 3;
    const int base = ((rq * 144 + 4 * rho) * NQ + g * 256 + cs * 64 + c8 * 8) * 2;
#pragma unroll
    for (int u = 0; u < 16; ++u) W[u] = BLD128(r, base, (32 * (u >> 2) + (u & 3)) * NQ * 2, AUX_W);
    const int base2 = ((rq * 144 + 128 + 2 * rho) * NQ + g * 256 + cs * 64 + c8 * 8) * 2;
    W[16] = BLD128(r, base2, 0, AUX_W); W[17] = BLD128(r, base2, NQ * 2, AUX_W);
    const unsigned b1v = __builtin_amdgcn_raw_buffer_load_b16(RSRC(b1, F * 2), (18 * b + 2 * c16 + (h4 & 1)) * 2, 0, 0);
    if (l < 8) flag[l] = 0u;
    LDS_WAIT();
    __builtin_amdgcn_s_barrier();
    asm volatile("" : "+v"(xa), "+v"(xb) :: "memory");
    TS(1);
    const bool act = l < 48;
    float v[12]; unsigned d[12];
#pragma unroll
    for (int i = 0; i < 4; ++i) { v[2 * i] = lo16(xa[i]); v[2 * i + 1] = hi16(xa[i]); }
#pragma unroll
    for (int i = 0; i < 2; ++i) { v[8 + 2 * i] = lo16(xb[i]); v[9 + 2 * i] = hi16(xb[i]); }
    layernorm(v, d, act);
    if (act) {
#pragma unroll
      for (int i = 0; i < 3; ++i) *(LAS u32x4*)(lds + L_X + l * 48 + 16 * i) = u32x4{d[4 * i], d[4 * i + 1], d[4 * i + 2], d[4 * i + 3]};
    }
    LDS_WAIT();
    u32x4 av[4]; u32x2 av2;
#pragma unroll
    for (int i = 0; i < 4; ++i) av[i] = *(LAS u32x4*)(lds + L_X + (rq * 144 + 32 * i + 4 * rho) * 4);
    av2 = *(LAS u32x2*)(lds + L_X + (rq * 144 + 128 + 2 * rho) * 4);
    f32x4 acc0 = {0.f, 0.f, 0.f, 0.f}, acc1 = acc0;
#pragma unroll
    for (int u = 0; u < 16; u += 2) {
      acc0 = mfma(msk & av[u >> 2][u & 3], W[u], acc0);
      acc1 = mfma(msk & av[u >> 2][(u & 3) + 1], W[u + 1], acc1);
    }
    acc0 = mfma(msk & av2[0], W[16], acc0);
    acc1 = mfma(msk & av2[1], W[17], acc1);
    const f32x2 pr = fold_pair(acc0 + acc1);
    tag = __builtin_amdgcn_readfirstlane(e0) * 1664525u + 1013904223u;
    BST128((u32x4{asu(pr[0]), tag, asu(pr[1]), tag}), rws,
           (l & 8) ? OOB : WS_QG + ((g * 4 + rq) * 128 + cs * 32 + 4 * c8 + 2 * (h4 & 1) + (h4 >> 1)) * 16, AUX_SC1);
    CFENCE();
    TS(2);
    layernorm(v, d, act);
    if (act) {
#pragma unroll
      for (int i = 0; i < 3; ++i) *(LAS u32x4*)(lds + L_Y + l * 48 + 16 * i) = u32x4{d[4 * i], d[4 * i + 1], d[4 * i + 2], d[4 * i + 3]};
    }
    if (QDELAY) __builtin_amdgcn_s_sleep(QDELAY);
    {
      u32x4 G[8];
      _Pragma("nounroll") for (unsigned it = 0; it < SPIN_CAP; ++it) {
        CFENCE();
        bool ok = true;
#pragma unroll
        for (int k = 0; k < 8; ++k) G[k] = BLD128(rws, WS_QG + g * 8192 + k * 1024 + l * 16, 0, AUX_SC1);
#pragma unroll
        for (int k = 0; k < 8; ++k) ok &= TAGOK(G[k]);
        if (__all(ok)) break;
      }
#pragma unroll
      for (int k = 0; k < 2; ++k)
        *(LAS unsigned*)(lds + L_Q + (k * 64 + l) * 4) = cvtpk((asf(G[k][0]) + asf(G[k + 2][0])) + (asf(G[k + 4][0]) + asf(G[k + 6][0])),
                                                                (asf(G[k][2]) + asf(G[k + 2][2])) + (asf(G[k + 4][2]) + asf(G[k + 6][2])));
    }
    if (l == 0) flag[FL_TAG] = tag;
    LDS_WAIT();
    if (l == 0) flag[FL_Q] = 1u;
    TS(3);
    lds_wait_ge(flag + FL_OPUB, 1);
    {
      u32x4 O0, O1, M0, M1;
      const int og = WS_OG + (((g * 16 + s) * 16 + c8) * 8 + rho) * 16;
      const int ml = WS_ML + ((g * 2 + (s >> 3)) * 16 + c8) * 16;
      _Pragma("nounroll") for (unsigned it = 0; it < SPIN_CAP; ++it) {
        CFENCE();
        O0 = BLD128(rws, og, 0, AUX_SC1); O1 = BLD128(rws, og + 1024, 0, AUX_SC1);
        M0 = BLD128(rws, ml, 0, AUX_SC1); M1 = BLD128(rws, ml + 128, 0, AUX_SC1);
        if (__all(TAGOK(O0) & TAGOK(O1) & TAGOK(M0) & TAGOK(M1))) break;
      }
      const float m0 = asf(M0[0]), m1 = asf(M1[0]);
      const float M = max8(fmaxf(m0, m1));
      const float e0_ = __builtin_amdgcn_exp2f((m0 - M) * LOG2E), e1_ = __builtin_amdgcn_exp2f((m1 - M) * LOG2E);
      const float L = sum8(e0_ * asf(M0[2]) + e1_ * asf(M1[2]));
      const float n0 = sum8(e0_ * asf(O0[0]) + e1_ * asf(O1[0])), n1 = sum8(e0_ * asf(O0[2]) + e1_ * asf(O1[2]));
      const float il = __builtin_amdgcn_rcpf(L);
      if (c8 == 0) *(LAS u32x2*)(lds + L_AO + rho * 8) = u32x2{cvtpk(n0 * il, n0 * il), cvtpk(n1 * il, n1 * il)};
    }
    TS(4);
    {
      u32x4 H[5];
      const int hg = c16 < 9 ? WS_HG + (h4 * 2304 + 9 * b + c16) * 16 : OOB;
      const int hg4 = h4 < 2 ? hg : OOB;
      _Pragma("nounroll") for (unsigned it = 0; it < SPIN_CAP; ++it) {
        CFENCE();
        bool ok = true;
#pragma unroll
        for (int k = 0; k < 5; ++k) H[k] = BLD128(rws, k < 4 ? hg : hg4, k * 4 * 2304 * 16, AUX_SC1);
#pragma unroll
        for (int k = 0; k < 4; ++k) ok &= TAGOK(H[k]);
        ok &= TAGOK(H[4]) | (h4 >= 2);
        if (__all(ok | (c16 >= 9))) break;
      }
      const float s0 = xrow_sum(((asf(H[0][0]) + asf(H[1][0])) + (asf(H[2][0]) + asf(H[3][0]))) + asf(H[4][0]));
      const float s1 = xrow_sum(((asf(H[0][2]) + asf(H[1][2])) + (asf(H[2][2]) + asf(H[3][2]))) + asf(H[4][2]));
      const float a = rb(rb((h4 & 1) ? s1 : s0) + lo16(b1v));
      const float z = a + 0.044715f * a * a * a;
      const float hh = a * __builtin_amdgcn_rcpf(1.f + __builtin_amdgcn_exp2f(z * (-2.f * 0.7978845608028654f * LOG2E)));
      if (l < 32) *(LAS unsigned*)(lds + L_AH + (2 * c16 + h4) * 4) = cvtpk(hh, hh);
    }
    LDS_WAIT();
    if (l == 0) flag[FL_A] = 1u;
    TS(5);
    fp = b;
  }
  if (fp >= 0) {
    const unsigned xo = __builtin_amdgcn_raw_buffer_load_b32(RSRC(x, D * 2), fp * 4, 0, 0);
    const unsigned bo = __builtin_amdgcn_raw_buffer_load_b32(RSRC(b2, D * 2), fp * 4, 0, 0);
    lds_wait_ge(flag + FL_OUT, 3);
    TS(6);
    u32x4 P[4];
    _Pragma("nounroll") for (unsigned it = 0; it < SPIN_CAP; ++it) {
      CFENCE();
      bool ok = true;
#pragma unroll
      for (int k = 0; k < 4; ++k) P[k] = BLD128(rws, WS_FG + (l * 288 + fp) * 16, k * 64 * 288 * 16, AUX_SC1);
#pragma unroll
      for (int k = 0; k < 4; ++k) ok &= TAGOK(P[k]);
      if (__all(ok)) break;
    }
    const float s0 = wave_sum((asf(P[0][0]) + asf(P[1][0])) + (asf(P[2][0]) + asf(P[3][0])));
    const float s1 = wave_sum((asf(P[0][2]) + asf(P[1][2])) + (asf(P[2][2]) + asf(P[3][2])));
    if (l == 0) {
      ((unsigned*)out)[fp] = cvtpk(lo16(xo) + s0 + lo16(bo), hi16(xo) + s1 + hi16(bo));
      if (fp == 0) __builtin_amdgcn_raw_buffer_store_b32(tag, rws, WS_E, 0, AUX_SC1);
    }
    TS(7);
  }
  TS_FLUSH();
}

__global__ void ka_k(unsigned char* ws) {
  u64 t_; asm volatile("s_memrealtime %0\n\ts_waitcnt lgkmcnt(0)" : "=s"(t_) :: "memory");
  if (threadIdx.x == 0 && blockIdx.x == 0) *(volatile unsigned*)(ws + WS_DBG - 64) = (unsigned)t_;
}
__global__ void kz_k(unsigned char* ws) {
  const unsigned ta = __builtin_amdgcn_readfirstlane(*(volatile unsigned*)(ws + WS_DBG - 64));
  for (unsigned it = 0; it < SPIN_CAP; ++it) { u64 t_; asm volatile("s_memrealtime %0\n\ts_waitcnt lgkmcnt(0)" : "=s"(t_) :: "memory"); if ((unsigned)t_ - ta >= 4000u) break; }
}
extern "C" void kernel_launch(void* const* d_in, const int* in_sizes, int n_in, void* d_out, int out_size, void* d_ws, size_t ws_size,
                              hipStream_t stream) {
  const u16* x = (const u16*)d_in[0];
  const u16* kc = (const u16*)d_in[1];
  const u16* vc = (const u16*)d_in[2];
  const u16* wq = (const u16*)d_in[3];
  const u16* wo = (const u16*)d_in[4];
  const u16* w1 = (const u16*)d_in[6];
  const u16* b1 = (const u16*)d_in[7];
  const u16* w2 = (const u16*)d_in[8];
  const u16* b2 = (const u16*)d_in[9];
  if (XV == 1 || XV == 3 || XV == 4) ka_k<<<1, 64, 0, stream>>>((unsigned char*)d_ws);
  if (XV == 5) ka_k<<<256, 768, 0, stream>>>((unsigned char*)d_ws);
  fused_k<<<256, NTHR, 0, stream>>>(x, wq, kc, vc, (unsigned char*)d_ws, w1, w2, wo, b1, b2, (u16*)d_out);
  if (XV == 2 || XV == 3) kz_k<<<1, 64, 0, stream>>>((unsigned char*)d_ws);
}
```

```cpp
#include <hip/hip_runtime.h>
#include <hip/hip_bf16.h>
#include <stdint.h>

typedef unsigned short u16;
typedef unsigned long long u64;
typedef __attribute__((ext_vector_type(8))) __bf16 bf16x8;
typedef __attribute__((ext_vector_type(2))) __bf16 bf16x2;
typedef __attribute__((ext_vector_type(4))) float f32x4;
typedef __attribute__((ext_vector_type(2))) float f32x2;
typedef __attribute__((ext_vector_type(4))) unsigned u32x4;
typedef __attribute__((ext_vector_type(2))) unsigned u32x2;
typedef __attribute__((ext_vector_type(8))) u64 u64x8;
typedef __attribute__((ext_vector_type(2))) u64 u64x2;
#define LAS __attribute__((address_space(3)))

constexpr int D = 576, NQ = 4096, SK = 16384, DH = 128, WIN = 4096, F = 4608;
constexpr int NWAVE = 11, NTHR = NWAVE * 64;
constexpr int R_ATT = 1  , R_W1 = 5, R_OUT = 6  ;

constexpr int T_A = 47, DT_A = 232;
constexpr int WIN_A = 8;
constexpr int T_1 = T_A + 32 * DT_A / 16, DT_1 = 200;
constexpr int T_W = T_1 + 6 * DT_1 / 16 + 8, DT_W = 275;
constexpr int T_Q = 300, T_QPUB = 100;
constexpr int T_O = T_1 + 160, T_H = T_1 + 6 * DT_1 / 16 + 104, T_STAG = 28;
constexpr int CODE_BYTES = 0x3a00;
constexpr int NT = 2, SC1 = 16;

constexpr int WS_E = 0;
constexpr int WS_QG = 4096;
constexpr int WS_HG = WS_QG + 16 * 4 * 128 * 16;
constexpr int WS_OG = WS_HG + 6 * 1152 * 16;
constexpr int WS_ML = WS_OG + 256 * 1024;
constexpr int WS_FG = WS_ML + 256 * 128;
constexpr int WS_END = WS_FG + 18 * 256 * 128;
constexpr int OOB = 0x7ff00000;
constexpr unsigned SPIN_CAP = 1u << 21;
constexpr unsigned EMPTY = 1u;

enum { FL_Q = 0, FL_ISS = 1, FL_ARR = 2, FL_OUT = 3, FL_TAG = 4, FL_W1 = 5, FL_Y = 6, FL_M = 7 };
constexpr int L_FLAG = 0;
constexpr int L_AO = 64;
constexpr int L_AH = L_AO + 64;
constexpr int L_Q = 256;
constexpr int L_X = L_Q + 512;
constexpr int L_Y = L_X + 2304;
constexpr int L_ML = L_Y + 2304;
constexpr int L_PART = L_ML + 64;
constexpr int L_K = (L_PART + 4 * 1024 + 255) & ~255;
constexpr int L_END = L_K + 16384;

#define RSRC(p, n) __builtin_amdgcn_make_buffer_rsrc((void*)(p), 0, (n), 0x00020000)
#define BLD128(r, off, soff, aux) __builtin_amdgcn_raw_buffer_load_b128((r), (off), (soff), (aux))
#define BST128(v, r, off, aux) __builtin_amdgcn_raw_buffer_store_b128((v), (r), (off), 0, (aux))
#define CFENCE() asm volatile("" ::: "memory")
#define LDS_WAIT() asm volatile("s_waitcnt lgkmcnt(0)" ::: "memory")
#define KEEP(v) asm volatile("" :: "v"(v))
#define SB() __builtin_amdgcn_sched_barrier(0)
#define LDS_INC(i) __hip_atomic_fetch_add((LAS unsigned*)(flag + (i)), 1u, __ATOMIC_RELAXED, __HIP_MEMORY_SCOPE_WORKGROUP)
#define WBAD(v) (((v)[1] ^ tag) | ((v)[3] ^ tag))
#define PBAD(v) (((v)[0] | (v)[1] | (v)[2] | (v)[3]) & 0x3ffu)
#define FCHK(k, m) "v_xor_b32 v[24+4*" #k "], %15, v[8+4*" #k "]\n\tv_xor_b32 v[25+4*" #k "], %16, v[9+4*" #k "]\n\t" \
                   "v_xor_b32 v[26+4*" #k "], %15, v[10+4*" #k "]\n\tv_xor_b32 v[27+4*" #k "], %16, v[11+4*" #k "]\n\t" \
                   "v_or3_b32 %9, v[24+4*" #k "], v[25+4*" #k "], v[26+4*" #k "]\n\tv_bitop3_b32 %9, %9, v[27+4*" #k "], %17 bitop3:0xa8\n\tv_cmp_ne_u32 %" #m ", 0, %9\n\t"
constexpr float LOG2E = 1.4426950408889634f;

__device__ __forceinline__ float asf(unsigned u) { return __builtin_bit_cast(float, u); }
__device__ __forceinline__ unsigned asu(float f) { return __builtin_bit_cast(unsigned, f); }
__device__ __forceinline__ unsigned cvtpk(float lo, float hi) {
  bf16x2 r = {(__bf16)lo, (__bf16)hi};
  unsigned u = __builtin_bit_cast(unsigned, r);
  asm("" : "+v"(u));
  return u;
}
__device__ __forceinline__ float lo16(unsigned u) { return asf(u << 16); }
__device__ __forceinline__ float hi16(unsigned u) { return asf(u & 0xffff0000u); }
__device__ __forceinline__ float rb(float f) { return hi16(cvtpk(f, f)); }
template <int CTRL> __device__ __forceinline__ float dpp(float x) {
  return asf(__builtin_amdgcn_mov_dpp(asu(x), CTRL, 0xf, 0xf, true));
}
constexpr int XOR1 = 0xB1, XOR2 = 0x4E, HMIR = 0x141, MIR = 0x140, ROR4 = 0x124, ROR8 = 0x128;
__device__ __forceinline__ float fmx(float a, float b) { float r; asm("v_max_f32 %0, %1, %2" : "=v"(r) : "v"(a), "v"(b)); return r; }
__device__ __forceinline__ float fadd(float a, float b) { float r; asm("v_add_f32 %0, %1, %2" : "=v"(r) : "v"(a), "v"(b)); return r; }
__device__ __forceinline__ float sum8(float x) { x += dpp<XOR1>(x); x += dpp<XOR2>(x); x += dpp<HMIR>(x); return x; }
__device__ __forceinline__ float xrow_sum(float x) {
  auto s = __builtin_amdgcn_permlane16_swap(asu(x), asu(x), false, false);
  x = asf(s[0]) + asf(s[1]);
  auto t = __builtin_amdgcn_permlane32_swap(asu(x), asu(x), false, false);
  return asf(t[0]) + asf(t[1]);
}
__device__ __forceinline__ float xrow_max(float x) {
  auto s = __builtin_amdgcn_permlane16_swap(asu(x), asu(x), false, false);
  x = fmx(asf(s[0]), asf(s[1]));
  auto t = __builtin_amdgcn_permlane32_swap(asu(x), asu(x), false, false);
  return fmx(asf(t[0]), asf(t[1]));
}
__device__ __forceinline__ float xrow_sum4(float v0, float v1, float v2, float v3) {
  auto a = __builtin_amdgcn_permlane32_swap(asu(v0), asu(v2), false, false);
  auto c = __builtin_amdgcn_permlane32_swap(asu(v1), asu(v3), false, false);
  const float p = asf(a[0]) + asf(a[1]), q = asf(c[0]) + asf(c[1]);
  auto e = __builtin_amdgcn_permlane16_swap(asu(p), asu(q), false, false);
  return asf(e[0]) + asf(e[1]);
}
__device__ __forceinline__ float wave_sum(float x) { x = sum8(x); x += dpp<MIR>(x); return xrow_sum(x); }
__device__ __forceinline__ f32x4 mfma(u32x4 a, u32x4 b, f32x4 c) {
  return __builtin_amdgcn_mfma_f32_16x16x32_bf16(__builtin_bit_cast(bf16x8, a), __builtin_bit_cast(bf16x8, b), c, 0, 0, 0);
}
__device__ __forceinline__ unsigned align_hi(float a) { return __builtin_amdgcn_update_dpp(asu(a), asu(a), ROR8, 0xc, 0xf, false); }
__device__ __forceinline__ unsigned pk1(float a, unsigned t) { return ((asu(a) + 0x200u) & 0xfffffc00u) | t; }
__device__ __forceinline__ u32x4 pack4(float a, float b, float c, float d, unsigned tag) {
  const unsigned ta = tag & 0x3ffu, tb = (tag >> 10) & 0x3ffu;
  return u32x4{pk1(a, ta), pk1(b, tb), pk1(c, ta), pk1(d, tb)};
}
__device__ __forceinline__ u32x4 untag(u32x4 v, unsigned tag) {
  const unsigned ta = tag & 0x3ffu, tb = (tag >> 10) & 0x3ffu;
  return u32x4{v[0] ^ ta, v[1] ^ tb, v[2] ^ ta, v[3] ^ tb};
}
__device__ __forceinline__ void lds_wait_ge(volatile LAS unsigned* p, unsigned v) {
  _Pragma("nounroll") for (unsigned i = 0; *p < v && i < SPIN_CAP; ++i) __builtin_amdgcn_s_sleep(1);
  CFENCE();
}
__device__ __forceinline__ unsigned now10ns() { return (unsigned)__builtin_amdgcn_s_memrealtime(); }
__device__ __forceinline__ void wait_until(unsigned t0, unsigned ticks) {
  _Pragma("nounroll") while ((int)(now10ns() - t0) < (int)ticks) __builtin_amdgcn_s_sleep(1);
}
__device__ __forceinline__ void pace(unsigned t0, int ticks) {
  asm volatile("1:\n\ts_memrealtime vcc\n\ts_waitcnt lgkmcnt(0)\n\ts_sub_u32 vcc_lo, vcc_lo, %0\n\ts_cmpk_lt_i32 vcc_lo, %1\n\ts_cbranch_scc1 1b"
               :: "s"(t0), "i"(ticks) : "scc", "vcc", "memory");
}
__device__ __forceinline__ void layernorm(float (&v)[12], unsigned (&d)[12], bool act) {
  float s = 0.f;
#pragma unroll
  for (int i = 0; i < 12; i += 4) s += (v[i] + v[i + 1]) + (v[i + 2] + v[i + 3]);
  const float mean = wave_sum(s) * (1.f / D);
  float q0 = 0.f, q1 = 0.f;
#pragma unroll
  for (int i = 0; i < 12; i += 2) { const float a = v[i] - mean, c = v[i + 1] - mean; q0 = fmaf(a, a, q0); q1 = fmaf(c, c, q1); }
  const float var = wave_sum(act ? q0 + q1 : 0.f) * (1.f / D);
  const float mb = rb(mean);
  const float den = rb(__builtin_amdgcn_sqrtf(rb(rb(var) + 1e-5f)));
  const float inv = act ? __builtin_amdgcn_rcpf(den) : 0.f;
#pragma unroll
  for (int i = 0; i < 12; i += 2) {
    const unsigned t = cvtpk(v[i] - mb, v[i + 1] - mb);
    const float a = lo16(t) * inv, c = hi16(t) * inv;
    d[i] = cvtpk(a, a); d[i + 1] = cvtpk(c, c);
    v[i] = hi16(d[i]); v[i + 1] = hi16(d[i + 1]);
  }
}

__global__ void __launch_bounds__(NTHR, 3) fused_k(const u16*, const u16*, const u16*, const u16*, unsigned char*,
                                                   const u16*, const u16*, const u16*, const u16*, const u16*, u16*) {
  u64x8 ka; u64x2 kb; u64 outp;
  asm volatile("s_load_dwordx16 %0, %3, 0x0\n\ts_load_dwordx4 %1, %3, 0x40\n\ts_load_dwordx2 %2, %3, 0x50"
               : "=&s"(ka), "=&s"(kb), "=&s"(outp) : "s"(__builtin_amdgcn_kernarg_segment_ptr()));
  SB();
  __shared__ __attribute__((aligned(16))) unsigned char lds_raw[L_END];
  LAS unsigned char* lds = (LAS unsigned char*)lds_raw;
  volatile LAS unsigned* flag = (volatile LAS unsigned*)(lds + L_FLAG);
  const int b = blockIdx.x, tid = threadIdx.x, w = __builtin_amdgcn_readfirstlane(tid >> 6), l = tid & 63;
  const int g = (b & 7) + 8 * (b >> 7), s = (b >> 3) & 15;
  const int gx = g ^ 1;
  const int jf = (b & 7) * 32 + (b >> 3);
  const int rho = l >> 3, c8 = l & 7, h4 = l >> 4, c16 = l & 15;
  unsigned pf0, pf1;
  {
    u64 pc;
    asm volatile("s_getpc_b64 %2\n\ts_cmp_lg_u32 %3, 6\n\ts_cbranch_scc1 1f\n\t"
                 "v_lshlrev_b32 %0, 7, %4\n\tv_min_u32 %1, %5, %0\n\tv_add_u32 %1, 0x2000, %1\n\t"
                 "global_load_dword %0, %0, %2\n\tglobal_load_dword %1, %1, %2\n1:"
                 : "=&v"(pf0), "=&v"(pf1), "=&s"(pc) : "s"(w), "v"(l), "s"(CODE_BYTES - 0x2000) : "scc", "memory");
  }
  flag[l] = l < 16 ? 0u : EMPTY;
  const int aw = w - R_ATT;
  int kvb = ((g * SK + (SK - WIN) + s * 256 + aw * 64) * DH * 2) | ((s ^ (int)(((unsigned)((b & 7) * 2 + (b >> 7) + 1) * 0x9E3779B1u) >> 28)) << 10);
  int krec = (unsigned)aw < 4u ? 16 * SK * DH * 2 : 0;
  const int rq = s >> 2, cs = s & 3;
  int l16 = l * 16, l24 = l * 24;
  int base = ((rq * 144 + 4 * rho) * NQ + gx * 256 + cs * 64 + c8 * 8) * 2;
  int base2 = ((rq * 144 + 128 + 2 * rho) * NQ + gx * 256 + cs * 64 + c8 * 8) * 2;
  asm volatile("" : "+v"(l16), "+v"(l24), "+v"(base), "+v"(base2) :: "memory");
  asm volatile("s_waitcnt lgkmcnt(0)" : "+s"(ka), "+s"(kb), "+s"(outp), "+s"(kvb), "+s"(krec) :: "memory");
  const u16 *x = (const u16*)ka[0], *wq = (const u16*)ka[1], *kc = (const u16*)ka[2], *vc = (const u16*)ka[3];
  unsigned char* ws = (unsigned char*)ka[4];
  const u16 *w1 = (const u16*)ka[5], *w2 = (const u16*)ka[6], *wo = (const u16*)ka[7], *b1 = (const u16*)kb[0], *b2 = (const u16*)kb[1];
  u64 t064;
  asm volatile("s_memrealtime %0" : "=s"(t064));
  auto rws = RSRC(ws, WS_END);

  u32x4 R[18], xa; u32x2 xb; unsigned e0;
  auto rk = RSRC(kc, krec);
  const u32x4 K0 = BLD128(rk, l16, kvb, NT);
  SB();
#pragma unroll
  for (int i = 0; i < 18; ++i) asm volatile("" : "=v"(R[i]));
  asm volatile("" : "=v"(xa), "=v"(xb), "=v"(e0));
  if (__builtin_expect(w == 0, 1)) {
    auto rx = RSRC(x, D * 2);
    xa = BLD128(rx, l24, 0, 0);
    xb = __builtin_amdgcn_raw_buffer_load_b64(rx, l24 + 16, 0, 0);
    e0 = __builtin_amdgcn_raw_buffer_load_b32(rws, WS_E, 0, SC1);
    SB();
    auto r = RSRC(wq, D * NQ * 2);
#pragma unroll
    for (int u = 0; u < 16; ++u) R[u] = BLD128(r, base, (32 * (u >> 2) + (u & 3)) * NQ * 2, NT);
    R[16] = BLD128(r, base2, 0, NT); R[17] = BLD128(r, base2, NQ * 2, NT);
  }
  __builtin_amdgcn_s_barrier();
  asm volatile("s_waitcnt lgkmcnt(0)" : "+s"(t064) :: "memory");
  unsigned t0 = (unsigned)t064;
  int w2nd = w;
  asm volatile("" : "+s"(w2nd) :: "memory");
  u32x4 msk;
#pragma unroll
  for (int r = 0; r < 4; ++r) msk[r] = ((c8 >> 1) == r) ? ((c8 & 1) ? 0xffff0000u : 0x0000ffffu) : 0u;
  unsigned tag = 0;
  int job = 0, stag = 0;
  unsigned b1r = 0;
  float xb2 = 0.f;
  const int idx = b >> 3, grp = idx < 16 ? 2 * (b & 7) + (idx >> 3) : 14 + 18 * (b & 7) + (idx >> 3);
  const int oc = grp * 32 + (idx & 7) * 4 + h4;
  const int hf = (18 * jf & ~3) + 4 * c8 + h4;

  if (__builtin_expect((unsigned)(w2nd - R_ATT) < 4u, 1)) {
    u32x4 KV[32], KF[16];
    {
      auto rv = RSRC(vc, 16 * SK * DH * 2);
      LAS unsigned char* kl = lds + L_K + aw * 4096;
      t0 += (2 * aw - 3) * DT_A >> 7;
#pragma unroll
      for (int i = 0; i < 32; ++i) {
        const int j = i - WIN_A;
        if (j >= 0 && j < 16) *(LAS u32x4*)(kl + (4 * h4 + (j & 3)) * 256 + ((c16 ^ (4 * h4 + (j & 3))) << 4)) = KV[j];
        else if (j >= 16) KEEP(KV[j]);
        if (i == 0) KV[0] = K0;
        else KV[i] = BLD128(i < 16 ? rk : rv, l16, kvb ^ ((i & 15) << 10), NT);
        if (i < 31) pace(t0, T_A + (((i + 1) * DT_A) >> 4));
        if (j >= 0 && j < 16 && (j & 3) == 3) {
#pragma unroll
          for (int k = 0; k < 4; ++k) KF[j - 3 + k] = *(LAS u32x4*)(kl + c16 * 256 + (((4 * k + h4) ^ c16) << 4));
        }
        if (i == 30) { if (l == 0) LDS_INC(FL_ISS); }
        if (i == 20) b1r = __builtin_amdgcn_raw_buffer_load_b16(RSRC(b1, F * 2), hf * 2, 0, 0);
        SB();
      }
    }
    CFENCE();
    lds_wait_ge(flag + FL_Q, 1);
    u32x4 Q[4];
#pragma unroll
    for (int t = 0; t < 4; ++t) Q[t] = *(LAS u32x4*)(lds + L_Q + (c16 >> 3) * 256 + t * 64 + h4 * 16);
    tag = __builtin_amdgcn_readfirstlane(flag[FL_TAG]);
    f32x4 S[4];
#pragma unroll
    for (int kt = 0; kt < 4; ++kt) S[kt] = mfma(KF[kt * 4], Q[0], f32x4{0.f, 0.f, 0.f, 0.f});
#pragma unroll
    for (int t = 1; t < 4; ++t)
#pragma unroll
      for (int kt = 0; kt < 4; ++kt) S[kt] = mfma(KF[kt * 4 + t], Q[t], S[kt]);
    float sc[16], m = -INFINITY;
#pragma unroll
    for (int i = 0; i < 16; i += 2) {
      const unsigned t = cvtpk(S[i >> 2][i & 3], S[i >> 2][(i & 3) + 1]);
      const unsigned u = cvtpk(lo16(t) * 0.08838834764831845f, hi16(t) * 0.08838834764831845f);
      sc[i] = lo16(u); sc[i + 1] = hi16(u);
      m = fmaxf(m, fmaxf(sc[i], sc[i + 1]));
    }
    m = xrow_max(m);
    float ls0 = 0.f, ls1 = 0.f;
    unsigned pp[16];
#pragma unroll
    for (int i = 0; i < 16; i += 2) {
      const float p0 = __builtin_amdgcn_exp2f((sc[i] - m) * LOG2E), p1 = __builtin_amdgcn_exp2f((sc[i + 1] - m) * LOG2E);
      ls0 += p0; ls1 += p1; pp[i] = cvtpk(p0, p0); pp[i + 1] = cvtpk(p1, p1);
    }
    const float lsum = xrow_sum(ls0 + ls1);
    if ((l & 55) == 0) *(LAS f32x2*)(lds + L_ML + (rho * 4 + aw) * 8) = f32x2{m, lsum};
    CFENCE();
    if (l == 0) LDS_INC(FL_M);
    f32x4 o0 = {0.f, 0.f, 0.f, 0.f}, o1 = o0;
#pragma unroll
    for (int i = 0; i < 10; i += 2) { o0 = mfma(msk & pp[i], KV[16 + i], o0); o1 = mfma(msk & pp[i + 1], KV[17 + i], o1); }
    lds_wait_ge(flag + FL_M, 4);
    float scale, M, L;
    {
      const f32x4 ma = *(LAS f32x4*)(lds + L_ML + (l >> 5) * 32), mc = *(LAS f32x4*)(lds + L_ML + (l >> 5) * 32 + 16);
      const float mine = *(LAS float*)(lds + L_ML + ((l >> 5) * 4 + aw) * 8);
      M = fmx(fmx(ma[0], ma[2]), fmx(mc[0], mc[2]));
      scale = __builtin_amdgcn_exp2f((mine - M) * LOG2E);
      L = (__builtin_amdgcn_exp2f((ma[0] - M) * LOG2E) * ma[1] + __builtin_amdgcn_exp2f((ma[2] - M) * LOG2E) * ma[3]) +
          (__builtin_amdgcn_exp2f((mc[0] - M) * LOG2E) * mc[1] + __builtin_amdgcn_exp2f((mc[2] - M) * LOG2E) * mc[3]);
      asm volatile("" : "+v"(L));
    }
    const int mo = (l & 28) ? OOB : WS_ML + (g * 16 + s) * 128 + (l >> 5) * 64 + (l & 3) * 16;
    SB();
#pragma unroll
    for (int i = 10; i < 16; i += 2) { o0 = mfma(msk & pp[i], KV[16 + i], o0); o1 = mfma(msk & pp[i + 1], KV[17 + i], o1); }
    *(LAS f32x4*)(lds + L_PART + aw * 1024 + ((h4 >> 1) * 128 + 8 * c16 + 4 * (h4 & 1)) * 4) = (o0 + o1) * scale;
    CFENCE();
    unsigned old = 0;
    if (l == 0) old = LDS_INC(FL_ARR);
    old = __builtin_amdgcn_readfirstlane(old);
    CFENCE();
    if (old == 3) {
      f32x4 pa[4];
#pragma unroll
      for (int i = 0; i < 4; ++i) pa[i] = *(LAS f32x4*)(lds + L_PART + i * 1024 + l * 16);
      BST128((u32x4{asu(M), tag, asu(L), tag}), rws, mo, SC1);
      const f32x4 o = (pa[0] + pa[1]) + (pa[2] + pa[3]);
      BST128(pack4(o[0], o[1], o[2], o[3], tag), rws, WS_OG + (g * 16 + s) * 1024 + l * 16, SC1);
      CFENCE();
    }
    job = old < 2 ? 2 : old == 2 ? 1 : 0;
    stag = old == 0 ? 0 : T_STAG;
  } else if (w2nd == R_W1) {
    const int r64 = (b >> 3) * 2 + (b & 1);
    lds_wait_ge(flag + FL_Y, 1);
    tag = __builtin_amdgcn_readfirstlane(flag[FL_TAG]);
    if (r64 < 54) {
      const int part = r64 / 9, slab = ((((b >> 1) + 1) & 3) * 9) + r64 - part * 9;
      auto r1 = RSRC(w1, D * F * 2);
      const int o1 = ((part * 96 + 24 * h4) * F + slab * 128 + c16 * 8) * 2;
      u32x4 ay[6], W1[24];
#pragma unroll
      for (int i = 0; i < 6; ++i) ay[i] = *(LAS u32x4*)(lds + L_Y + (part * 96 + 24 * h4 + 4 * i) * 4);
      const int ho = l < 32 ? WS_HG + (part * 1152 + slab * 32 + 2 * c16 + h4) * 16 : OOB;
      lds_wait_ge(flag + FL_ISS, 4);
#pragma unroll
      for (int i = 0; i < 24; ++i) {
        W1[i] = BLD128(r1, o1, i * F * 2, NT);
        if (i == 21) flag[FL_W1] = 1u;
        if ((i & 1) && i < 23) pace(t0, T_1 + (((i + 1) * DT_1) >> 6) - (DT_1 >> 6));
        SB();
      }
      f32x4 a0 = {0.f, 0.f, 0.f, 0.f}, a1 = a0;
#pragma unroll
      for (int i = 0; i < 24; i += 2) { a0 = mfma(msk & ay[i >> 2][i & 3], W1[i], a0); a1 = mfma(msk & ay[i >> 2][(i & 3) + 1], W1[i + 1], a1); }
      a0 += a1;
      BST128(pack4(a0[0], a0[1], a0[2], a0[3], tag), rws, ho, SC1);
      CFENCE();
    } else {
      lds_wait_ge(flag + FL_ISS, 4);
      flag[FL_W1] = 1u;
      if (b == 255) {
        _Pragma("nounroll") for (unsigned it = 0; it < SPIN_CAP; ++it) {
          CFENCE();
          unsigned bad = 0;
#pragma unroll
          for (int k = 0; k < 4; ++k) { const u32x4 G = BLD128(rws, WS_QG + l * 512, k * 32768, SC1); bad |= WBAD(G); }
          if (__all(bad == 0)) break;
          __builtin_amdgcn_s_sleep(16);
        }
        __builtin_amdgcn_raw_buffer_store_b32(tag, rws, l == 0 ? WS_E : OOB, 0, SC1);
      }
    }
  } else if (w2nd >= R_OUT) {
    const int ow = w - R_OUT;
    asm volatile("s_waitcnt vmcnt(0)" : "+v"(pf0), "+v"(pf1) :: "memory");
    auto r2 = RSRC(w2, F * D * 2);
    auto ro = RSRC(wo, NQ * D * 2);
    auto r2b = RSRC(w2 + 64, ow < 4 ? F * D * 2 - 128 : 0);
    auto rob = RSRC(wo + 64, ow < 4 ? NQ * D * 2 - 128 : 0);
    const int col = (ow * 128 + c8 * 8) * 2;
    const int o2 = (18 * jf + 2 * rho) * D * 2 + col;
    const int o2c = rho < 2 ? (18 * jf + 16 + rho) * D * 2 + col : OOB;
    const int oo = (gx * 256 + 16 * s + 2 * rho) * D * 2 + col;
    const int fo = ((l & 8) || (ow == 4 && l >= 32)) ? OOB : WS_FG + ((ow * 4 + (l >> 5) * 2 + (c8 >> 2)) * 256 + b) * 128 + ((c8 & 3) * 2 + (h4 & 1)) * 16;
    lds_wait_ge(flag + FL_W1, 1);
    t0 += (ow - 2) * DT_W / 80;
    pace(t0, T_W);
    u32x4 W[10];
#pragma unroll
    for (int i = 0; i < 10; ++i) {
      const int u = i >> 1;
      W[i] = u < 2 ? BLD128((i & 1) ? rob : ro, oo + u * D * 2, 0, NT) : BLD128((i & 1) ? r2b : r2, u < 4 ? o2 + (u - 2) * D * 2 : o2c, 0, NT);
      if (i < 8) pace(t0, T_W + (((i < 7 ? (i + 1) * 16 : 7 * 16 + 4) * DT_W) >> 8));
      SB();
    }
    tag = __builtin_amdgcn_readfirstlane(flag[FL_TAG]);
    CFENCE();
    u32x2 ao, ah; unsigned ah2;
    _Pragma("nounroll") for (unsigned it = 0; it < SPIN_CAP; ++it) {
      ao = *(volatile LAS u32x2*)(lds + L_AO + rho * 8);
      if (!__any(min(ao[0] ^ EMPTY, ao[1] ^ EMPTY) == 0u)) break;
    }
    f32x4 oc0, oc1;
    { const u32x4 A0 = msk & ao[0], A1 = msk & ao[1];
      oc0 = mfma(A0, W[0], f32x4{0.f, 0.f, 0.f, 0.f}); oc1 = mfma(A0, W[1], f32x4{0.f, 0.f, 0.f, 0.f});
      oc0 = mfma(A1, W[2], oc0); oc1 = mfma(A1, W[3], oc1); }
    _Pragma("nounroll") for (unsigned it = 0; it < SPIN_CAP; ++it) {
      ah = *(volatile LAS u32x2*)(lds + L_AH + rho * 8);
      ah2 = *(volatile LAS unsigned*)(lds + L_AH + 64 + (rho & 1) * 4);
      if (!__any(min(min(ah[0] ^ EMPTY, ah[1] ^ EMPTY), ah2 ^ EMPTY) == 0u)) break;
    }
    { const u32x4 A0 = msk & ah[0], A1 = msk & ah[1], A2 = msk & ah2;
      oc0 = mfma(A0, W[4], oc0); oc1 = mfma(A0, W[5], oc1);
      oc0 = mfma(A1, W[6], oc0); oc1 = mfma(A1, W[7], oc1);
      oc0 = mfma(A2, W[8], oc0); oc1 = mfma(A2, W[9], oc1); }
    float fs[4];
#pragma unroll
    for (int r = 0; r < 4; ++r) {
      auto t = __builtin_amdgcn_permlane32_swap(align_hi(oc0[r]), align_hi(oc1[r]), false, false);
      fs[r] = fadd(asf(t[0]), asf(t[1]));
    }
    BST128(pack4(fs[0], fs[1], fs[2], fs[3], tag), rws, fo, SC1);
    CFENCE();
    if (l == 0) LDS_INC(FL_OUT);
  } else {
    __builtin_amdgcn_s_setprio(3);
    asm volatile("" : "+v"(xa), "+v"(xb) :: "memory");
    const bool act = l < 48;
    float v[12]; unsigned d[12];
#pragma unroll
    for (int i = 0; i < 4; ++i) { v[2 * i] = lo16(xa[i]); v[2 * i + 1] = hi16(xa[i]); }
#pragma unroll
    for (int i = 0; i < 2; ++i) { v[8 + 2 * i] = lo16(xb[i]); v[9 + 2 * i] = hi16(xb[i]); }
    layernorm(v, d, act);
    if (act) {
#pragma unroll
      for (int i = 0; i < 3; ++i) *(LAS u32x4*)(lds + L_X + l * 48 + 16 * i) = u32x4{d[4 * i], d[4 * i + 1], d[4 * i + 2], d[4 * i + 3]};
    }
    LDS_WAIT();
    u32x4 av[4]; u32x2 av2;
#pragma unroll
    for (int i = 0; i < 4; ++i) av[i] = *(LAS u32x4*)(lds + L_X + (rq * 144 + 32 * i + 4 * rho) * 4);
    av2 = *(LAS u32x2*)(lds + L_X + (rq * 144 + 128 + 2 * rho) * 4);
    f32x4 acc0 = {0.f, 0.f, 0.f, 0.f}, acc1 = acc0;
#pragma unroll
    for (int u = 0; u < 16; u += 2) {
      acc0 = mfma(msk & av[u >> 2][u & 3], R[u], acc0);
      acc1 = mfma(msk & av[u >> 2][(u & 3) + 1], R[u + 1], acc1);
    }
    acc0 = mfma(msk & av2[0], R[16], acc0);
    acc1 = mfma(msk & av2[1], R[17], acc1);
    acc0 += acc1;
    tag = __builtin_amdgcn_readfirstlane(e0) * 1664525u + 1013904223u;
    {
      auto s0 = __builtin_amdgcn_permlane32_swap(align_hi(acc0[0]), align_hi(acc0[2]), false, false);
      auto s1 = __builtin_amdgcn_permlane32_swap(align_hi(acc0[1]), align_hi(acc0[3]), false, false);
      BST128((u32x4{asu(asf(s0[0]) + asf(s0[1])), tag, asu(asf(s1[0]) + asf(s1[1])), tag}), rws,
             (l & 8) ? OOB : WS_QG + ((gx * 4 + rq) * 128 + cs * 32 + 4 * c8 + 2 * (h4 & 1) + (h4 >> 1)) * 16, SC1);
    }
    CFENCE();
    const unsigned tpub = now10ns();
    layernorm(v, d, act);
    if (act) {
#pragma unroll
      for (int i = 0; i < 3; ++i) *(LAS u32x4*)(lds + L_Y + l * 48 + 16 * i) = u32x4{d[4 * i], d[4 * i + 1], d[4 * i + 2], d[4 * i + 3]};
    }
    CFENCE();
    flag[FL_TAG] = tag;
    flag[FL_Y] = 1u;
    wait_until(t0, T_Q);
    wait_until(tpub, T_QPUB);
    {
      u32x4 G[8];
      _Pragma("nounroll") for (unsigned it = 0; it < SPIN_CAP; ++it) {
        CFENCE();
        unsigned bad = 0;
#pragma unroll
        for (int k = 0; k < 8; ++k) G[k] = BLD128(rws, WS_QG + g * 8192 + k * 1024 + l * 16, 0, SC1);
#pragma unroll
        for (int k = 0; k < 8; ++k) bad |= WBAD(G[k]);
        if (__all(bad == 0)) break;
        __builtin_amdgcn_s_sleep(4);
      }
#pragma unroll
      for (int k = 0; k < 2; ++k)
        *(LAS unsigned*)(lds + L_Q + (k * 64 + l) * 4) = cvtpk((asf(G[k][0]) + asf(G[k + 2][0])) + (asf(G[k + 4][0]) + asf(G[k + 6][0])),
                                                                (asf(G[k][2]) + asf(G[k + 2][2])) + (asf(G[k + 4][2]) + asf(G[k + 6][2])));
    }
    CFENCE();
    flag[FL_Q] = 1u;
    xb2 = lo16(__builtin_amdgcn_raw_buffer_load_b16(RSRC(x, D * 2), oc * 2, 0, 0)) + lo16(__builtin_amdgcn_raw_buffer_load_b16(RSRC(b2, D * 2), oc * 2, 0, 0));
    asm volatile("" : "+v"(xb2));
    job = 5;
  }

  if (job & 1) {
    const int og = WS_OG + (gx * 16 + (l >> 2)) * 1024 + s * 64 + (l & 3) * 16;
    const int mg = WS_ML + (gx * 16 + (l >> 2)) * 128 + (s >> 3) * 64;
    wait_until(t0, T_O + stag);
    _Pragma("nounroll") for (unsigned it = 0; it < SPIN_CAP; ++it) {
      CFENCE();
      if (*(volatile LAS unsigned*)(lds + L_AO) != EMPTY) break;
      const u32x4 O = untag(BLD128(rws, og, 0, SC1), tag), ML = BLD128(rws, mg, 0, SC1);
      if (__all((PBAD(O) | WBAD(ML)) == 0)) {
        const float m = asf(ML[0]);
        float M = fmx(m, dpp<ROR4>(m)); M = fmx(M, dpp<ROR8>(M)); M = xrow_max(M);
        const float e = __builtin_amdgcn_exp2f((m - M) * LOG2E);
        const f32x4 o = __builtin_bit_cast(f32x4, O) * e;
        float L = e * asf(ML[2]);
        float n = xrow_sum4(o[0], o[1], o[2], o[3]);
        L = xrow_sum(L);
        n += dpp<ROR8>(n); L += dpp<ROR8>(L);
        n += dpp<ROR4>(n); L += dpp<ROR4>(L);
        n *= __builtin_amdgcn_rcpf(L);
        *(LAS unsigned*)(lds + L_AO + ((l & 3) * 4 + h4) * 4) = cvtpk(n, n);
        break;
      }
      __builtin_amdgcn_s_sleep(1);
    }
  }
  if (job & 2) {
    const int fi = hf - 18 * jf;
    const bool use = rho < 6 && c8 < 5;
    const int hg = use ? WS_HG + (rho * 1152 + (18 * jf >> 2) + c8) * 16 : OOB;
    const unsigned ltag = use ? tag : 0u;
    float b1v = lo16(b1r);
    int ha = L_AH + ((unsigned)fi < 18u ? fi : 31) * 4;
    asm volatile("" : "+v"(b1v), "+v"(ha));
    wait_until(t0, T_H + stag);
    _Pragma("nounroll") for (unsigned it = 0; it < SPIN_CAP; ++it) {
      CFENCE();
      if (*(volatile LAS unsigned*)(lds + L_AH) != EMPTY) break;
      const u32x4 H = untag(BLD128(rws, hg, 0, SC1), ltag);
      if (__all(PBAD(H) == 0)) {
        const f32x4 hv = __builtin_bit_cast(f32x4, H);
        float t = xrow_sum4(hv[0], hv[1], hv[2], hv[3]);
        t += dpp<ROR8>(t);
        const float a = rb(rb(t) + b1v);
        constexpr float C1 = -2.f * 0.7978845608028654f * LOG2E, C3 = C1 * 0.044715f;
        const float hh = a * __builtin_amdgcn_rcpf(1.f + __builtin_amdgcn_exp2f(a * fmaf(a * a, C3, C1)));
        *(LAS unsigned*)(lds + ha) = cvtpk(hh, hh);
        break;
      }
      __builtin_amdgcn_s_sleep(1);
    }
  }
  if (job & 4) {
    if (grp < 18) {
      int fo = WS_FG + (grp * 256 + l) * 128 + (idx & 7) * 16, fo1 = fo + 8192, fo2 = fo + 16384, fo3 = fo + 24576;
      int oo = c16 == 0 ? oc * 2 : OOB;
      asm volatile("" : "+v"(fo), "+v"(fo1), "+v"(fo2), "+v"(fo3), "+v"(xb2), "+v"(oo));
      auto ro = RSRC(outp, D * 2);
      lds_wait_ge(flag + FL_OUT, 5);
      __builtin_amdgcn_s_sleep(10);
      f32x4 P[4];
      {
        const u32x4 rd = {(unsigned)ka[4], (unsigned)(ka[4] >> 32) & 0xffffu, (unsigned)WS_END, 0x00020000u};
        u64 m0 = ~0ull, m1 = ~0ull, m2 = ~0ull, m3 = ~0ull; unsigned n = SPIN_CAP, t;
        asm volatile(
            "1:\n\t"
            "s_mov_b64 exec, %4\n\tbuffer_load_dwordx4 v[8:11], %10, %14, 0 offen sc1\n\t"
            "s_mov_b64 exec, %5\n\tbuffer_load_dwordx4 v[12:15], %11, %14, 0 offen sc1\n\t"
            "s_mov_b64 exec, %6\n\tbuffer_load_dwordx4 v[16:19], %12, %14, 0 offen sc1\n\t"
            "s_mov_b64 exec, %7\n\tbuffer_load_dwordx4 v[20:23], %13, %14, 0 offen sc1\n\t"
            "s_mov_b64 exec, -1\n\t"
            "s_sub_u32 %8, %8, 1\n\ts_waitcnt vmcnt(0)\n\ts_cbranch_scc1 2f\n\t"
            FCHK(0, 4) FCHK(1, 5) FCHK(2, 6) FCHK(3, 7)
            "s_or_b64 vcc, %4, %5\n\ts_or_b64 vcc, vcc, %6\n\ts_nop 0\n\ts_or_b64 vcc, vcc, %7\n\t"
            "s_cbranch_scc1 1b\n"
            "2:"
            : "=&{v[24:27]}"(P[0]), "=&{v[28:31]}"(P[1]), "=&{v[32:35]}"(P[2]), "=&{v[36:39]}"(P[3]), "+s"(m0), "+s"(m1), "+s"(m2), "+s"(m3), "+s"(n), "=&v"(t)
            : "v"(fo), "v"(fo1), "v"(fo2), "v"(fo3), "s"(rd), "s"(tag & 0x3ffu), "s"((tag >> 10) & 0x3ffu), "s"(0x3ffu)
            : "vcc", "scc", "memory", "v8", "v9", "v10", "v11", "v12", "v13", "v14", "v15", "v16", "v17", "v18", "v19", "v20", "v21", "v22", "v23");
      }
      const f32x4 t = (P[0] + P[1]) + (P[2] + P[3]);
      float r = xrow_sum4(t[0], t[1], t[2], t[3]);
      r = sum8(r); r += dpp<MIR>(r);
      __builtin_amdgcn_raw_buffer_store_b16((u16)cvtpk(r + xb2, r + xb2), ro, oo, 0, 0);
    }
  }
}

extern "C" void kernel_launch(void* const* d_in, const int* in_sizes, int n_in, void* d_out, int out_size, void* d_ws, size_t ws_size,
                              hipStream_t stream) {
  const u16* x = (const u16*)d_in[0];
  const u16* kc = (const u16*)d_in[1];
  const u16* vc = (const u16*)d_in[2];
  const u16* wq = (const u16*)d_in[3];
  const u16* wo = (const u16*)d_in[4];
  const u16* w1 = (const u16*)d_in[6];
  const u16* b1 = (const u16*)d_in[7];
  const u16* w2 = (const u16*)d_in[8];
  const u16* b2 = (const u16*)d_in[9];
  fused_k<<<256, NTHR, 0, stream>>>(x, wq, kc, vc, (unsigned char*)d_ws, w1, w2, wo, b1, b2, (u16*)d_out);
}
```

```cpp
#include <hip/hip_runtime.h>
#include <hip/hip_bf16.h>
#include <stdint.h>

typedef unsigned short u16;
typedef unsigned long long u64;
typedef __attribute__((ext_vector_type(8))) __bf16 bf16x8;
typedef __attribute__((ext_vector_type(2))) __bf16 bf16x2;
typedef __attribute__((ext_vector_type(4))) float f32x4;
typedef __attribute__((ext_vector_type(2))) float f32x2;
typedef __attribute__((ext_vector_type(4))) unsigned u32x4;
typedef __attribute__((ext_vector_type(2))) unsigned u32x2;
typedef __attribute__((ext_vector_type(8))) u64 u64x8;
typedef __attribute__((ext_vector_type(2))) u64 u64x2;
#define LAS __attribute__((address_space(3)))

constexpr int D = 576, NQ = 4096, SK = 16384, DH = 128, WIN = 4096, F = 4608;
constexpr int NWAVE = 11, NTHR = NWAVE * 64;
constexpr int R_ATT = 1  , R_W1 = 5, R_OUT = 6  ;

constexpr int T_A = 49, DT_A = 226;
constexpr int WIN_A = 12;
constexpr int T_1 = T_A + 32 * DT_A / 16, DT_1 = 200;
constexpr int T_W = T_1 + 6 * DT_1 / 16 + 10, DT_W = 275;
constexpr int T_Q = 300, T_QPUB = 100;
constexpr int T_O = T_1 + 177, T_H = T_1 + 6 * DT_1 / 16 + 114, T_STAG = 28;
constexpr int CODE_BYTES = 0x3a00;
constexpr int NT = 2, SC1 = 16;

constexpr int WS_E = 0;
constexpr int WS_QG = 4096;
constexpr int WS_HG = WS_QG + 16 * 4 * 128 * 16;
constexpr int WS_OG = WS_HG + 6 * 1152 * 16;
constexpr int WS_ML = WS_OG + 256 * 1024;
constexpr int WS_FG = WS_ML + 256 * 128;
constexpr int WS_END = WS_FG + 18 * 256 * 128;
constexpr int OOB = 0x7ff00000;
constexpr unsigned SPIN_CAP = 1u << 21;
constexpr unsigned EMPTY = 1u;

enum { FL_Q = 0, FL_ISS = 1, FL_ARR = 2, FL_OUT = 3, FL_TAG = 4, FL_W1 = 5, FL_Y = 6, FL_M = 7 };
constexpr int L_FLAG = 0;
constexpr int L_AO = 64;
constexpr int L_AH = L_AO + 64;
constexpr int L_Q = 256;
constexpr int L_X = L_Q + 512;
constexpr int L_Y = L_X + 2304;
constexpr int L_ML = L_Y + 2304;
constexpr int L_PART = L_ML + 64;
constexpr int L_K = (L_PART + 4 * 1024 + 255) & ~255;
constexpr int L_END = L_K + 16384;

#define RSRC(p, n) __builtin_amdgcn_make_buffer_rsrc((void*)(p), 0, (n), 0x00020000)
#define BLD128(r, off, soff, aux) __builtin_amdgcn_raw_buffer_load_b128((r), (off), (soff), (aux))
#define BST128(v, r, off, aux) __builtin_amdgcn_raw_buffer_store_b128((v), (r), (off), 0, (aux))
#define CFENCE() asm volatile("" ::: "memory")
#define LDS_WAIT() asm volatile("s_waitcnt lgkmcnt(0)" ::: "memory")
#define KEEP(v) asm volatile("" :: "v"(v))
#define SB() __builtin_amdgcn_sched_barrier(0)
#define LDS_INC(i) __hip_atomic_fetch_add((LAS unsigned*)(flag + (i)), 1u, __ATOMIC_RELAXED, __HIP_MEMORY_SCOPE_WORKGROUP)
#define WBAD(v) (((v)[1] ^ tag) | ((v)[3] ^ tag))
#define PBAD(v) (((v)[0] | (v)[1] | (v)[2] | (v)[3]) & 0x3ffu)
#define QBAD(v) (((v)[0] | (v)[1] | (v)[2] | (v)[3]) & 0xffu)
#define FCHK(k, m) "v_xor_b32 v[24+4*" #k "], %15, v[8+4*" #k "]\n\tv_xor_b32 v[25+4*" #k "], %16, v[9+4*" #k "]\n\t" \
                   "v_xor_b32 v[26+4*" #k "], %15, v[10+4*" #k "]\n\tv_xor_b32 v[27+4*" #k "], %16, v[11+4*" #k "]\n\t" \
                   "v_or3_b32 %9, v[24+4*" #k "], v[25+4*" #k "], v[26+4*" #k "]\n\tv_bitop3_b32 %9, %9, v[27+4*" #k "], %17 bitop3:0xa8\n\tv_cmp_ne_u32 %" #m ", 0, %9\n\t"
constexpr float LOG2E = 1.4426950408889634f;

__device__ __forceinline__ float asf(unsigned u) { return __builtin_bit_cast(float, u); }
__device__ __forceinline__ unsigned asu(float f) { return __builtin_bit_cast(unsigned, f); }
__device__ __forceinline__ unsigned cvtpk(float lo, float hi) {
  bf16x2 r = {(__bf16)lo, (__bf16)hi};
  unsigned u = __builtin_bit_cast(unsigned, r);
  asm("" : "+v"(u));
  return u;
}
__device__ __forceinline__ float lo16(unsigned u) { return asf(u << 16); }
__device__ __forceinline__ float hi16(unsigned u) { return asf(u & 0xffff0000u); }
__device__ __forceinline__ float rb(float f) { return hi16(cvtpk(f, f)); }
template <int CTRL> __device__ __forceinline__ float dpp(float x) {
  return asf(__builtin_amdgcn_mov_dpp(asu(x), CTRL, 0xf, 0xf, true));
}
constexpr int XOR1 = 0xB1, XOR2 = 0x4E, HMIR = 0x141, MIR = 0x140, ROR4 = 0x124, ROR8 = 0x128;
__device__ __forceinline__ float fmx(float a, float b) { float r; asm("v_max_f32 %0, %1, %2" : "=v"(r) : "v"(a), "v"(b)); return r; }
__device__ __forceinline__ float fadd(float a, float b) { float r; asm("v_add_f32 %0, %1, %2" : "=v"(r) : "v"(a), "v"(b)); return r; }
__device__ __forceinline__ float sum8(float x) { x += dpp<XOR1>(x); x += dpp<XOR2>(x); x += dpp<HMIR>(x); return x; }
__device__ __forceinline__ float xrow_sum(float x) {
  auto s = __builtin_amdgcn_permlane16_swap(asu(x), asu(x), false, false);
  x = asf(s[0]) + asf(s[1]);
  auto t = __builtin_amdgcn_permlane32_swap(asu(x), asu(x), false, false);
  return asf(t[0]) + asf(t[1]);
}
__device__ __forceinline__ float xrow_max(float x) {
  auto s = __builtin_amdgcn_permlane16_swap(asu(x), asu(x), false, false);
  x = fmx(asf(s[0]), asf(s[1]));
  auto t = __builtin_amdgcn_permlane32_swap(asu(x), asu(x), false, false);
  return fmx(asf(t[0]), asf(t[1]));
}
__device__ __forceinline__ float xrow_sum4(float v0, float v1, float v2, float v3) {
  auto a = __builtin_amdgcn_permlane32_swap(asu(v0), asu(v2), false, false);
  auto c = __builtin_amdgcn_permlane32_swap(asu(v1), asu(v3), false, false);
  const float p = asf(a[0]) + asf(a[1]), q = asf(c[0]) + asf(c[1]);
  auto e = __builtin_amdgcn_permlane16_swap(asu(p), asu(q), false, false);
  return asf(e[0]) + asf(e[1]);
}
__device__ __forceinline__ float wave_sum(float x) { x = sum8(x); x += dpp<MIR>(x); return xrow_sum(x); }
__device__ __forceinline__ f32x4 mfma(u32x4 a, u32x4 b, f32x4 c) {
  return __builtin_amdgcn_mfma_f32_16x16x32_bf16(__builtin_bit_cast(bf16x8, a), __builtin_bit_cast(bf16x8, b), c, 0, 0, 0);
}
__device__ __forceinline__ unsigned align_hi(float a) { return __builtin_amdgcn_update_dpp(asu(a), asu(a), ROR8, 0xc, 0xf, false); }
template <int NB> __device__ __forceinline__ unsigned pk1(float a, unsigned t) { return ((asu(a) + (1u << (NB - 1))) & (~0u << NB)) | t; }
template <int NB = 10> __device__ __forceinline__ u32x4 pack4(float a, float b, float c, float d, unsigned tag) {
  const unsigned ta = tag & ~(~0u << NB), tb = (tag >> NB) & ~(~0u << NB);
  return u32x4{pk1<NB>(a, ta), pk1<NB>(b, tb), pk1<NB>(c, ta), pk1<NB>(d, tb)};
}
template <int NB = 10> __device__ __forceinline__ u32x4 untag(u32x4 v, unsigned tag) {
  const unsigned ta = tag & ~(~0u << NB), tb = (tag >> NB) & ~(~0u << NB);
  return u32x4{v[0] ^ ta, v[1] ^ tb, v[2] ^ ta, v[3] ^ tb};
}
__device__ __forceinline__ void lds_wait_ge(volatile LAS unsigned* p, unsigned v) {
  _Pragma("nounroll") for (unsigned i = 0; *p < v && i < SPIN_CAP; ++i) __builtin_amdgcn_s_sleep(1);
  CFENCE();
}
__device__ __forceinline__ unsigned now10ns() { return (unsigned)__builtin_amdgcn_s_memrealtime(); }
__device__ __forceinline__ void wait_until(unsigned t0, unsigned ticks) {
  _Pragma("nounroll") while ((int)(now10ns() - t0) < (int)ticks) __builtin_amdgcn_s_sleep(1);
}
__device__ __forceinline__ void pace(unsigned t0, int ticks) {
  asm volatile("1:\n\ts_memrealtime vcc\n\ts_waitcnt lgkmcnt(0)\n\ts_sub_u32 vcc_lo, vcc_lo, %0\n\ts_cmpk_lt_i32 vcc_lo, %1\n\ts_cbranch_scc1 1b"
               :: "s"(t0), "i"(ticks) : "scc", "vcc", "memory");
}
__device__ __forceinline__ void layernorm(float (&v)[12], unsigned (&d)[12], bool act) {
  float s = 0.f;
#pragma unroll
  for (int i = 0; i < 12; i += 4) s += (v[i] + v[i + 1]) + (v[i + 2] + v[i + 3]);
  const float mean = wave_sum(s) * (1.f / D);
  float q0 = 0.f, q1 = 0.f;
#pragma unroll
  for (int i = 0; i < 12; i += 2) { const float a = v[i] - mean, c = v[i + 1] - mean; q0 = fmaf(a, a, q0); q1 = fmaf(c, c, q1); }
  const float var = wave_sum(act ? q0 + q1 : 0.f) * (1.f / D);
  const float mb = rb(mean);
  const float den = rb(__builtin_amdgcn_sqrtf(rb(rb(var) + 1e-5f)));
  const float inv = act ? __builtin_amdgcn_rcpf(den) : 0.f;
#pragma unroll
  for (int i = 0; i < 12; i += 2) {
    const unsigned t = cvtpk(v[i] - mb, v[i + 1] - mb);
    const float a = lo16(t) * inv, c = hi16(t) * inv;
    d[i] = cvtpk(a, a); d[i + 1] = cvtpk(c, c);
    v[i] = hi16(d[i]); v[i + 1] = hi16(d[i + 1]);
  }
}

__global__ void __launch_bounds__(NTHR, 3) fused_k(const u16*, const u16*, const u16*, const u16*, unsigned char*,
                                                   const u16*, const u16*, const u16*, const u16*, const u16*, u16*) {
  u64x8 ka; u64x2 kb; u64 outp;
  asm volatile("s_load_dwordx16 %0, %3, 0x0\n\ts_load_dwordx4 %1, %3, 0x40\n\ts_load_dwordx2 %2, %3, 0x50"
               : "=&s"(ka), "=&s"(kb), "=&s"(outp) : "s"(__builtin_amdgcn_kernarg_segment_ptr()));
  SB();
  __shared__ __attribute__((aligned(16))) unsigned char lds_raw[L_END];
  LAS unsigned char* lds = (LAS unsigned char*)lds_raw;
  volatile LAS unsigned* flag = (volatile LAS unsigned*)(lds + L_FLAG);
  const int b = blockIdx.x, tid = threadIdx.x, w = __builtin_amdgcn_readfirstlane(tid >> 6), l = tid & 63;
  const int g = (b & 7) + 8 * (b >> 7), s = (b >> 3) & 15;
  const int gx = g ^ 1;
  const int jf = (b & 7) * 32 + (b >> 3);
  const int rho = l >> 3, c8 = l & 7, h4 = l >> 4, c16 = l & 15;
  unsigned pf0, pf1;
  {
    u64 pc;
    asm volatile("s_getpc_b64 %2\n\ts_cmp_lg_u32 %3, 6\n\ts_cbranch_scc1 1f\n\t"
                 "v_lshlrev_b32 %0, 7, %4\n\tv_min_u32 %1, %5, %0\n\tv_add_u32 %1, 0x2000, %1\n\t"
                 "global_load_dword %0, %0, %2\n\tglobal_load_dword %1, %1, %2\n1:"
                 : "=&v"(pf0), "=&v"(pf1), "=&s"(pc) : "s"(w), "v"(l), "s"(CODE_BYTES - 0x2000) : "scc", "memory");
  }
  flag[l] = l < 16 ? 0u : EMPTY;
  const int aw = w - R_ATT;
  int kvb = ((g * SK + (SK - WIN) + s * 256 + aw * 64) * DH * 2) | ((s ^ (int)(((unsigned)((b & 7) * 2 + (b >> 7) + 1) * 0x9E3779B1u) >> 28)) << 10);
  int krec = (unsigned)aw < 4u ? 16 * SK * DH * 2 : 0;
  const int rq = s >> 2, cs = s & 3;
  int l16 = l * 16, l24 = l * 24;
  int base = ((rq * 144 + 4 * rho) * NQ + gx * 256 + cs * 64 + c8 * 8) * 2;
  int base2 = ((rq * 144 + 128 + 2 * rho) * NQ + gx * 256 + cs * 64 + c8 * 8) * 2;
  asm volatile("" : "+v"(l16), "+v"(l24), "+v"(base), "+v"(base2) :: "memory");
  asm volatile("s_waitcnt lgkmcnt(0)" : "+s"(ka), "+s"(kb), "+s"(outp), "+s"(kvb), "+s"(krec) :: "memory");
  const u16 *x = (const u16*)ka[0], *wq = (const u16*)ka[1], *kc = (const u16*)ka[2], *vc = (const u16*)ka[3];
  unsigned char* ws = (unsigned char*)ka[4];
  const u16 *w1 = (const u16*)ka[5], *w2 = (const u16*)ka[6], *wo = (const u16*)ka[7], *b1 = (const u16*)kb[0], *b2 = (const u16*)kb[1];
  u64 t064;
  asm volatile("s_memrealtime %0" : "=s"(t064));
  auto rws = RSRC(ws, WS_END);

  u32x4 R[18], xa; u32x2 xb; unsigned e0;
  auto rk = RSRC(kc, krec);
  const u32x4 K0 = BLD128(rk, l16, kvb, NT);
  SB();
#pragma unroll
  for (int i = 0; i < 18; ++i) asm volatile("" : "=v"(R[i]));
  asm volatile("" : "=v"(xa), "=v"(xb), "=v"(e0));
  if (__builtin_expect(w == 0, 1)) {
    auto rx = RSRC(x, D * 2);
    xa = BLD128(rx, l24, 0, 0);
    xb = __builtin_amdgcn_raw_buffer_load_b64(rx, l24 + 16, 0, 0);
    e0 = __builtin_amdgcn_raw_buffer_load_b32(rws, WS_E, 0, SC1);
    SB();
    auto r = RSRC(wq, D * NQ * 2);
#pragma unroll
    for (int u = 0; u < 16; ++u) R[u] = BLD128(r, base, (32 * (u >> 2) + (u & 3)) * NQ * 2, NT);
    R[16] = BLD128(r, base2, 0, NT); R[17] = BLD128(r, base2, NQ * 2, NT);
  }
  __builtin_amdgcn_s_barrier();
  asm volatile("s_waitcnt lgkmcnt(0)" : "+s"(t064) :: "memory");
  unsigned t0 = (unsigned)t064;
  int w2nd = w;
  asm volatile("" : "+s"(w2nd) :: "memory");
  u32x4 msk;
#pragma unroll
  for (int r = 0; r < 4; ++r) msk[r] = ((c8 >> 1) == r) ? ((c8 & 1) ? 0xffff0000u : 0x0000ffffu) : 0u;
  unsigned tag = 0;
  int job = 0, stag = 0;
  unsigned b1r = 0;
  float xb2 = 0.f;
  const int idx = b >> 3, grp = idx < 16 ? 2 * (b & 7) + (idx >> 3) : 14 + 18 * (b & 7) + (idx >> 3);
  const int oc = grp * 32 + (idx & 7) * 4 + h4;
  const int hf = (18 * jf & ~3) + 4 * c8 + h4;

  if (__builtin_expect((unsigned)(w2nd - R_ATT) < 4u, 1)) {
    u32x4 KV[32], KF[16];
    {
      auto rv = RSRC(vc, 16 * SK * DH * 2);
      LAS unsigned char* kl = lds + L_K + aw * 4096;
      t0 += (2 * aw - 3) * DT_A >> 7;
#pragma unroll
      for (int i = 0; i < 32; ++i) {
        const int j = i - WIN_A;
        if (j >= 0 && j < 16) *(LAS u32x4*)(kl + (4 * h4 + (j & 3)) * 256 + ((c16 ^ (4 * h4 + (j & 3))) << 4)) = KV[j];
        else if (j >= 16) KEEP(KV[j]);
        if (i == 0) KV[0] = K0;
        else KV[i] = BLD128(i < 16 ? rk : rv, l16, kvb ^ ((i & 15) << 10), NT);
        if (i < 31) pace(t0, T_A + (((i + 1) * DT_A) >> 4));
        if (j >= 0 && j < 16 && (j & 3) == 3) {
#pragma unroll
          for (int k = 0; k < 4; ++k) KF[j - 3 + k] = *(LAS u32x4*)(kl + c16 * 256 + (((4 * k + h4) ^ c16) << 4));
        }
        if (i == 30) { if (l == 0) LDS_INC(FL_ISS); }
        if (i == 20) b1r = __builtin_amdgcn_raw_buffer_load_b16(RSRC(b1, F * 2), hf * 2, 0, 0);
        SB();
      }
    }
    CFENCE();
    lds_wait_ge(flag + FL_Q, 1);
    u32x4 Q[4];
#pragma unroll
    for (int t = 0; t < 4; ++t) Q[t] = *(LAS u32x4*)(lds + L_Q + (c16 >> 3) * 256 + t * 64 + h4 * 16);
    tag = __builtin_amdgcn_readfirstlane(flag[FL_TAG]);
    f32x4 S[4];
#pragma unroll
    for (int kt = 0; kt < 4; ++kt) S[kt] = mfma(KF[kt * 4], Q[0], f32x4{0.f, 0.f, 0.f, 0.f});
#pragma unroll
    for (int t = 1; t < 4; ++t)
#pragma unroll
      for (int kt = 0; kt < 4; ++kt) S[kt] = mfma(KF[kt * 4 + t], Q[t], S[kt]);
    float sc[16], m = -INFINITY;
#pragma unroll
    for (int i = 0; i < 16; i += 2) {
      const unsigned t = cvtpk(S[i >> 2][i & 3], S[i >> 2][(i & 3) + 1]);
      const unsigned u = cvtpk(lo16(t) * 0.08838834764831845f, hi16(t) * 0.08838834764831845f);
      sc[i] = lo16(u); sc[i + 1] = hi16(u);
      m = fmaxf(m, fmaxf(sc[i], sc[i + 1]));
    }
    m = xrow_max(m);
    float ls0 = 0.f, ls1 = 0.f;
    unsigned pp[16];
#pragma unroll
    for (int i = 0; i < 16; i += 2) {
      const float p0 = __builtin_amdgcn_exp2f((sc[i] - m) * LOG2E), p1 = __builtin_amdgcn_exp2f((sc[i + 1] - m) * LOG2E);
      ls0 += p0; ls1 += p1; pp[i] = cvtpk(p0, p0); pp[i + 1] = cvtpk(p1, p1);
    }
    const float lsum = xrow_sum(ls0 + ls1);
    if ((l & 55) == 0) *(LAS f32x2*)(lds + L_ML + (rho * 4 + aw) * 8) = f32x2{m, lsum};
    CFENCE();
    if (l == 0) LDS_INC(FL_M);
    f32x4 o0 = {0.f, 0.f, 0.f, 0.f}, o1 = o0;
#pragma unroll
    for (int i = 0; i < 10; i += 2) { o0 = mfma(msk & pp[i], KV[16 + i], o0); o1 = mfma(msk & pp[i + 1], KV[17 + i], o1); }
    lds_wait_ge(flag + FL_M, 4);
    float scale, M, L;
    {
      const f32x4 ma = *(LAS f32x4*)(lds + L_ML + (l >> 5) * 32), mc = *(LAS f32x4*)(lds + L_ML + (l >> 5) * 32 + 16);
      const float mine = *(LAS float*)(lds + L_ML + ((l >> 5) * 4 + aw) * 8);
      M = fmx(fmx(ma[0], ma[2]), fmx(mc[0], mc[2]));
      scale = __builtin_amdgcn_exp2f((mine - M) * LOG2E);
      L = (__builtin_amdgcn_exp2f((ma[0] - M) * LOG2E) * ma[1] + __builtin_amdgcn_exp2f((ma[2] - M) * LOG2E) * ma[3]) +
          (__builtin_amdgcn_exp2f((mc[0] - M) * LOG2E) * mc[1] + __builtin_amdgcn_exp2f((mc[2] - M) * LOG2E) * mc[3]);
      asm volatile("" : "+v"(L));
    }
    const int mo = (l & 28) ? OOB : WS_ML + (g * 16 + s) * 128 + (l >> 5) * 64 + (l & 3) * 16;
    SB();
#pragma unroll
    for (int i = 10; i < 16; i += 2) { o0 = mfma(msk & pp[i], KV[16 + i], o0); o1 = mfma(msk & pp[i + 1], KV[17 + i], o1); }
    *(LAS f32x4*)(lds + L_PART + aw * 1024 + ((h4 >> 1) * 128 + 8 * c16 + 4 * (h4 & 1)) * 4) = (o0 + o1) * scale;
    CFENCE();
    unsigned old = 0;
    if (l == 0) old = LDS_INC(FL_ARR);
    old = __builtin_amdgcn_readfirstlane(old);
    CFENCE();
    if (old == 3) {
      f32x4 pa[4];
#pragma unroll
      for (int i = 0; i < 4; ++i) pa[i] = *(LAS f32x4*)(lds + L_PART + i * 1024 + l * 16);
      const f32x4 o = (pa[0] + pa[1]) + (pa[2] + pa[3]);
      BST128((u32x4{asu(M), tag, asu(L), tag}), rws, mo, SC1);
      BST128(pack4(o[0], o[1], o[2], o[3], tag), rws, WS_OG + (g * 16 + s) * 1024 + l * 16, SC1);
      CFENCE();
    }
    job = old < 2 ? 2 : old == 2 ? 1 : 0;
    stag = old == 0 ? 0 : T_STAG;
  } else if (w2nd == R_W1) {
    const int r64 = (b >> 3) * 2 + (b & 1);
    lds_wait_ge(flag + FL_Y, 1);
    tag = __builtin_amdgcn_readfirstlane(flag[FL_TAG]);
    if (r64 < 54) {
      const int part = r64 / 9, slab = ((((b >> 1) + 1) & 3) * 9) + r64 - part * 9;
      auto r1 = RSRC(w1, D * F * 2);
      const int o1 = ((part * 96 + 24 * h4) * F + slab * 128 + c16 * 8) * 2;
      u32x4 ay[6], W1[24];
#pragma unroll
      for (int i = 0; i < 6; ++i) ay[i] = *(LAS u32x4*)(lds + L_Y + (part * 96 + 24 * h4 + 4 * i) * 4);
      const int ho = l < 32 ? WS_HG + (part * 1152 + slab * 32 + 2 * c16 + h4) * 16 : OOB;
      lds_wait_ge(flag + FL_ISS, 4);
#pragma unroll
      for (int i = 0; i < 24; ++i) {
        W1[i] = BLD128(r1, o1, i * F * 2, NT);
        if (i == 21) flag[FL_W1] = 1u;
        if ((i & 1) && i < 23) pace(t0, T_1 + (((i + 1) * DT_1) >> 6) - (DT_1 >> 6));
        SB();
      }
      f32x4 a0 = {0.f, 0.f, 0.f, 0.f}, a1 = a0;
#pragma unroll
      for (int i = 0; i < 24; i += 2) { a0 = mfma(msk & ay[i >> 2][i & 3], W1[i], a0); a1 = mfma(msk & ay[i >> 2][(i & 3) + 1], W1[i + 1], a1); }
      a0 += a1;
      BST128(pack4(a0[0], a0[1], a0[2], a0[3], tag), rws, ho, SC1);
      CFENCE();
    } else {
      lds_wait_ge(flag + FL_ISS, 4);
      flag[FL_W1] = 1u;
      if (b == 255) {
        _Pragma("nounroll") for (unsigned it = 0; it < SPIN_CAP; ++it) {
          CFENCE();
          unsigned bad = 0;
#pragma unroll
          for (int k = 0; k < 4; ++k) {
            const u32x4 G = untag<8>(BLD128(rws, WS_QG + l * 256, k * 16384, SC1), tag), G2 = untag<8>(BLD128(rws, WS_QG + l * 256 + 16, k * 16384, SC1), tag);
            bad |= QBAD(G) | QBAD(G2);
          }
          if (__all(bad == 0)) break;
          __builtin_amdgcn_s_sleep(16);
        }
        __builtin_amdgcn_raw_buffer_store_b32(tag, rws, l == 0 ? WS_E : OOB, 0, SC1);
      }
    }
  } else if (w2nd >= R_OUT) {
    const int ow = w - R_OUT;
    asm volatile("s_waitcnt vmcnt(0)" : "+v"(pf0), "+v"(pf1) :: "memory");
    auto r2 = RSRC(w2, F * D * 2);
    auto ro = RSRC(wo, NQ * D * 2);
    auto r2b = RSRC(w2 + 64, ow < 4 ? F * D * 2 - 128 : 0);
    auto rob = RSRC(wo + 64, ow < 4 ? NQ * D * 2 - 128 : 0);
    const int col = (ow * 128 + c8 * 8) * 2;
    const int o2 = (18 * jf + 2 * rho) * D * 2 + col;
    const int o2c = rho < 2 ? (18 * jf + 16 + rho) * D * 2 + col : OOB;
    const int oo = (gx * 256 + 16 * s + 2 * rho) * D * 2 + col;
    const int fo = ((l & 8) || (ow == 4 && l >= 32)) ? OOB : WS_FG + ((ow * 4 + (l >> 5) * 2 + (c8 >> 2)) * 256 + b) * 128 + ((c8 & 3) * 2 + (h4 & 1)) * 16;
    lds_wait_ge(flag + FL_W1, 1);
    t0 += (((ow + 1) % 5) * 2 - 3) * DT_W / 80;
    pace(t0, T_W);
    u32x4 W[10];
#pragma unroll
    for (int i = 0; i < 10; ++i) {
      const int u = i >> 1;
      W[i] = u < 2 ? BLD128((i & 1) ? rob : ro, oo + u * D * 2, 0, NT) : BLD128((i & 1) ? r2b : r2, u < 4 ? o2 + (u - 2) * D * 2 : o2c, 0, NT);
      if ((i & 1) && i < 6) pace(t0, T_W + (((i + 1) * DT_W) >> 4));
      SB();
    }
    tag = __builtin_amdgcn_readfirstlane(flag[FL_TAG]);
    CFENCE();
    u32x2 ao, ah; unsigned ah2;
    _Pragma("nounroll") for (unsigned it = 0; it < SPIN_CAP; ++it) {
      ao = *(volatile LAS u32x2*)(lds + L_AO + rho * 8);
      if (!__any(min(ao[0] ^ EMPTY, ao[1] ^ EMPTY) == 0u)) break;
    }
    f32x4 oc0, oc1;
    { const u32x4 A0 = msk & ao[0], A1 = msk & ao[1];
      oc0 = mfma(A0, W[0], f32x4{0.f, 0.f, 0.f, 0.f}); oc1 = mfma(A0, W[1], f32x4{0.f, 0.f, 0.f, 0.f});
      oc0 = mfma(A1, W[2], oc0); oc1 = mfma(A1, W[3], oc1); }
    _Pragma("nounroll") for (unsigned it = 0; it < SPIN_CAP; ++it) {
      ah = *(volatile LAS u32x2*)(lds + L_AH + rho * 8);
      ah2 = *(volatile LAS unsigned*)(lds + L_AH + 64 + (rho & 1) * 4);
      if (!__any(min(min(ah[0] ^ EMPTY, ah[1] ^ EMPTY), ah2 ^ EMPTY) == 0u)) break;
    }
    { const u32x4 A0 = msk & ah[0], A1 = msk & ah[1], A2 = msk & ah2;
      oc0 = mfma(A0, W[4], oc0); oc1 = mfma(A0, W[5], oc1);
      oc0 = mfma(A1, W[6], oc0); oc1 = mfma(A1, W[7], oc1);
      oc0 = mfma(A2, W[8], oc0); oc1 = mfma(A2, W[9], oc1); }
    float fs[4];
#pragma unroll
    for (int r = 0; r < 4; ++r) {
      auto t = __builtin_amdgcn_permlane32_swap(align_hi(oc0[r]), align_hi(oc1[r]), false, false);
      fs[r] = fadd(asf(t[0]), asf(t[1]));
    }
    BST128(pack4(fs[0], fs[1], fs[2], fs[3], tag), rws, fo, SC1);
    CFENCE();
    if (l == 0) LDS_INC(FL_OUT);
  } else {
    __builtin_amdgcn_s_setprio(3);
    asm volatile("" : "+v"(xa), "+v"(xb) :: "memory");
    const bool act = l < 48;
    float v[12]; unsigned d[12];
#pragma unroll
    for (int i = 0; i < 4; ++i) { v[2 * i] = lo16(xa[i]); v[2 * i + 1] = hi16(xa[i]); }
#pragma unroll
    for (int i = 0; i < 2; ++i) { v[8 + 2 * i] = lo16(xb[i]); v[9 + 2 * i] = hi16(xb[i]); }
    layernorm(v, d, act);
    if (act) {
#pragma unroll
      for (int i = 0; i < 3; ++i) *(LAS u32x4*)(lds + L_X + l * 48 + 16 * i) = u32x4{d[4 * i], d[4 * i + 1], d[4 * i + 2], d[4 * i + 3]};
    }
    LDS_WAIT();
    u32x4 av[4]; u32x2 av2;
#pragma unroll
    for (int i = 0; i < 4; ++i) av[i] = *(LAS u32x4*)(lds + L_X + (rq * 144 + 32 * i + 4 * rho) * 4);
    av2 = *(LAS u32x2*)(lds + L_X + (rq * 144 + 128 + 2 * rho) * 4);
    f32x4 acc0 = {0.f, 0.f, 0.f, 0.f}, acc1 = acc0;
#pragma unroll
    for (int u = 0; u < 16; u += 2) {
      acc0 = mfma(msk & av[u >> 2][u & 3], R[u], acc0);
      acc1 = mfma(msk & av[u >> 2][(u & 3) + 1], R[u + 1], acc1);
    }
    acc0 = mfma(msk & av2[0], R[16], acc0);
    acc1 = mfma(msk & av2[1], R[17], acc1);
    acc0 += acc1;
    tag = __builtin_amdgcn_readfirstlane(e0) * 1664525u + 1013904223u;
    {
      float qs[4];
#pragma unroll
      for (int r = 0; r < 4; ++r) { const unsigned u = align_hi(acc0[r]); auto t = __builtin_amdgcn_permlane32_swap(u, u, false, false); qs[r] = asf(t[0]) + asf(t[1]); }
      BST128(pack4<8>(qs[0], qs[1], qs[2], qs[3], tag), rws, (l & 40) ? OOB : WS_QG + ((gx * 4 + rq) * 64 + cs * 16 + 2 * c8 + h4) * 16, SC1);
    }
    CFENCE();
    const unsigned tpub = now10ns();
    layernorm(v, d, act);
    if (act) {
#pragma unroll
      for (int i = 0; i < 3; ++i) *(LAS u32x4*)(lds + L_Y + l * 48 + 16 * i) = u32x4{d[4 * i], d[4 * i + 1], d[4 * i + 2], d[4 * i + 3]};
    }
    CFENCE();
    flag[FL_TAG] = tag;
    flag[FL_Y] = 1u;
    wait_until(t0, T_Q);
    wait_until(tpub, T_QPUB);
    {
      u32x4 G[4];
      _Pragma("nounroll") for (unsigned it = 0; it < SPIN_CAP; ++it) {
        CFENCE();
#pragma unroll
        for (int k = 0; k < 4; ++k) G[k] = untag<8>(BLD128(rws, WS_QG + g * 4096 + k * 1024 + l * 16, 0, SC1), tag);
        if (__all((QBAD(G[0]) | QBAD(G[1]) | QBAD(G[2]) | QBAD(G[3])) == 0)) break;
        __builtin_amdgcn_s_sleep(4);
      }
      const f32x4 qv = (__builtin_bit_cast(f32x4, G[0]) + __builtin_bit_cast(f32x4, G[1])) + (__builtin_bit_cast(f32x4, G[2]) + __builtin_bit_cast(f32x4, G[3]));
      *(LAS u32x2*)(lds + L_Q + l * 8) = u32x2{cvtpk(qv[0], qv[1]), cvtpk(qv[2], qv[3])};
    }
    CFENCE();
    flag[FL_Q] = 1u;
    xb2 = lo16(__builtin_amdgcn_raw_buffer_load_b16(RSRC(x, D * 2), oc * 2, 0, 0)) + lo16(__builtin_amdgcn_raw_buffer_load_b16(RSRC(b2, D * 2), oc * 2, 0, 0));
    asm volatile("" : "+v"(xb2));
    job = 5;
  }

  if (job & 1) {
    const int og = WS_OG + (gx * 16 + (l >> 2)) * 1024 + s * 64 + (l & 3) * 16;
    const int mg = WS_ML + (gx * 16 + (l >> 2)) * 128 + (s >> 3) * 64;
    wait_until(t0, T_O + stag);
    _Pragma("nounroll") for (unsigned it = 0; it < SPIN_CAP; ++it) {
      CFENCE();
      if (*(volatile LAS unsigned*)(lds + L_AO) != EMPTY) break;
      const u32x4 O = untag(BLD128(rws, og, 0, SC1), tag), ML = BLD128(rws, mg, 0, SC1);
      if (__all((PBAD(O) | WBAD(ML)) == 0)) {
        const float m = asf(ML[0]);
        float M = fmx(m, dpp<ROR4>(m)); M = fmx(M, dpp<ROR8>(M)); M = xrow_max(M);
        const float e = __builtin_amdgcn_exp2f((m - M) * LOG2E);
        const f32x4 o = __builtin_bit_cast(f32x4, O) * e;
        float L = e * asf(ML[2]);
        float n = xrow_sum4(o[0], o[1], o[2], o[3]);
        L = xrow_sum(L);
        n += dpp<ROR8>(n); L += dpp<ROR8>(L);
        n += dpp<ROR4>(n); L += dpp<ROR4>(L);
        n *= __builtin_amdgcn_rcpf(L);
        *(LAS unsigned*)(lds + L_AO + ((l & 3) * 4 + h4) * 4) = cvtpk(n, n);
        break;
      }
      __builtin_amdgcn_s_sleep(1);
    }
  }
  if (job & 2) {
    const int fi = hf - 18 * jf;
    const bool use = rho < 6 && c8 < 5;
    const int hg = use ? WS_HG + (rho * 1152 + (18 * jf >> 2) + c8) * 16 : OOB;
    const unsigned ltag = use ? tag : 0u;
    float b1v = lo16(b1r);
    int ha = L_AH + ((unsigned)fi < 18u ? fi : 31) * 4;
    asm volatile("" : "+v"(b1v), "+v"(ha));
    wait_until(t0, T_H + stag);
    _Pragma("nounroll") for (unsigned it = 0; it < SPIN_CAP; ++it) {
      CFENCE();
      if (*(volatile LAS unsigned*)(lds + L_AH) != EMPTY) break;
      const u32x4 H = untag(BLD128(rws, hg, 0, SC1), ltag);
      if (__all(PBAD(H) == 0)) {
        const f32x4 hv = __builtin_bit_cast(f32x4, H);
        float t = xrow_sum4(hv[0], hv[1], hv[2], hv[3]);
        t += dpp<ROR8>(t);
        const float a = rb(rb(t) + b1v);
        constexpr float C1 = -2.f * 0.7978845608028654f * LOG2E, C3 = C1 * 0.044715f;
        const float hh = a * __builtin_amdgcn_rcpf(1.f + __builtin_amdgcn_exp2f(a * fmaf(a * a, C3, C1)));
        *(LAS unsigned*)(lds + ha) = cvtpk(hh, hh);
        break;
      }
      __builtin_amdgcn_s_sleep(1);
    }
  }
  if (job & 4) {
    if (grp < 18) {
      int fo = WS_FG + (grp * 256 + l) * 128 + (idx & 7) * 16, fo1 = fo + 8192, fo2 = fo + 16384, fo3 = fo + 24576;
      int oo = c16 == 0 ? oc * 2 : OOB;
      asm volatile("" : "+v"(fo), "+v"(fo1), "+v"(fo2), "+v"(fo3), "+v"(xb2), "+v"(oo));
      auto ro = RSRC(outp, D * 2);
      lds_wait_ge(flag + FL_OUT, 5);
      __builtin_amdgcn_s_sleep(10);
      f32x4 P[4];
      {
        const u32x4 rd = {(unsigned)ka[4], (unsigned)(ka[4] >> 32) & 0xffffu, (unsigned)WS_END, 0x00020000u};
        u64 m0 = ~0ull, m1 = ~0ull, m2 = ~0ull, m3 = ~0ull; unsigned n = SPIN_CAP, t;
        asm volatile(
            "1:\n\t"
            "s_mov_b64 exec, %4\n\tbuffer_load_dwordx4 v[8:11], %10, %14, 0 offen sc1\n\t"
            "s_mov_b64 exec, %5\n\tbuffer_load_dwordx4 v[12:15], %11, %14, 0 offen sc1\n\t"
            "s_mov_b64 exec, %6\n\tbuffer_load_dwordx4 v[16:19], %12, %14, 0 offen sc1\n\t"
            "s_mov_b64 exec, %7\n\tbuffer_load_dwordx4 v[20:23], %13, %14, 0 offen sc1\n\t"
            "s_mov_b64 exec, -1\n\t"
            "s_sub_u32 %8, %8, 1\n\ts_waitcnt vmcnt(0)\n\ts_cbranch_scc1 2f\n\t"
            FCHK(0, 4) FCHK(1, 5) FCHK(2, 6) FCHK(3, 7)
            "s_or_b64 vcc, %4, %5\n\ts_or_b64 vcc, vcc, %6\n\ts_nop 0\n\ts_or_b64 vcc, vcc, %7\n\t"
            "s_cbranch_scc1 1b\n"
            "2:"
            : "=&{v[24:27]}"(P[0]), "=&{v[28:31]}"(P[1]), "=&{v[32:35]}"(P[2]), "=&{v[36:39]}"(P[3]), "+s"(m0), "+s"(m1), "+s"(m2), "+s"(m3), "+s"(n), "=&v"(t)
            : "v"(fo), "v"(fo1), "v"(fo2), "v"(fo3), "s"(rd), "s"(tag & 0x3ffu), "s"((tag >> 10) & 0x3ffu), "s"(0x3ffu)
            : "vcc", "scc", "memory", "v8", "v9", "v10", "v11", "v12", "v13", "v14", "v15", "v16", "v17", "v18", "v19", "v20", "v21", "v22", "v23");
      }
      const f32x4 t = (P[0] + P[1]) + (P[2] + P[3]);
      float r = xrow_sum4(t[0], t[1], t[2], t[3]);
      r = sum8(r); r += dpp<MIR>(r);
      __builtin_amdgcn_raw_buffer_store_b16((u16)cvtpk(r + xb2, r + xb2), ro, oo, 0, 0);
    }
  }
}

extern "C" void kernel_launch(void* const* d_in, const int* in_sizes, int n_in, void* d_out, int out_size, void* d_ws, size_t ws_size,
                              hipStream_t stream) {
  const u16* x = (const u16*)d_in[0];
  const u16* kc = (const u16*)d_in[1];
  const u16* vc = (const u16*)d_in[2];
  const u16* wq = (const u16*)d_in[3];
  const u16* wo = (const u16*)d_in[4];
  const u16* w1 = (const u16*)d_in[6];
  const u16* b1 = (const u16*)d_in[7];
  const u16* w2 = (const u16*)d_in[8];
  const u16* b2 = (const u16*)d_in[9];
  fused_k<<<256, NTHR, 0, stream>>>(x, wq, kc, vc, (unsigned char*)d_ws, w1, w2, wo, b1, b2, (u16*)d_out);
}
```

```cpp
#include <hip/hip_runtime.h>
#include <hip/hip_bf16.h>
#include <stdint.h>

typedef unsigned short u16;
typedef unsigned long long u64;
typedef __attribute__((ext_vector_type(8))) __bf16 bf16x8;
typedef __attribute__((ext_vector_type(2))) __bf16 bf16x2;
typedef __attribute__((ext_vector_type(4))) float f32x4;
typedef __attribute__((ext_vector_type(2))) float f32x2;
typedef __attribute__((ext_vector_type(4))) unsigned u32x4;
typedef __attribute__((ext_vector_type(2))) unsigned u32x2;
typedef __attribute__((ext_vector_type(8))) u64 u64x8;
typedef __attribute__((ext_vector_type(2))) u64 u64x2;
#define LAS __attribute__((address_space(3)))

constexpr int D = 576, NQ = 4096, SK = 16384, DH = 128, WIN = 4096, F = 4608;
constexpr int NWAVE = 11, NTHR = NWAVE * 64;
constexpr int R_ATT = 1  , R_W1 = 5, R_OUT = 6  ;

constexpr int T_A = 49, DT_A = 223;
constexpr int WIN_A = 12;
constexpr int T_1 = T_A + 32 * DT_A / 16, DT_1 = 200;
constexpr int T_W = T_1 + 6 * DT_1 / 16 + 10, DT_W = 275;
constexpr int T_Q = 300, T_QPUB = 100;
constexpr int T_O = T_1 + 177, T_H = T_1 + 6 * DT_1 / 16 + 114, T_STAG = 28;
constexpr int CODE_BYTES = 0x3a00;
constexpr int NT = 2, SC1 = 16;

constexpr int WS_E = 0;
constexpr int WS_QG = 4096;
constexpr int WS_HG = WS_QG + 16 * 4 * 64 * 16;
constexpr int WS_OG = WS_HG + 6 * 1152 * 16;
constexpr int WS_ML = WS_OG + 256 * 1024;
constexpr int WS_FG = WS_ML + 256 * 128;
constexpr int WS_END = WS_FG + 18 * 256 * 128;
constexpr int OOB = 0x7ff00000;
constexpr unsigned SPIN_CAP = 1u << 21;
constexpr unsigned EMPTY = 1u;

enum { FL_Q = 0, FL_ISS = 1, FL_ARR = 2, FL_OUT = 3, FL_TAG = 4, FL_W1 = 5, FL_Y = 6, FL_M = 7 };
constexpr int L_FLAG = 0;
constexpr int L_AO = 64;
constexpr int L_AH = L_AO + 64;
constexpr int L_Q = 256;
constexpr int L_X = L_Q + 512;
constexpr int L_Y = L_X + 2304;
constexpr int L_ML = L_Y + 2304;
constexpr int L_PART = L_ML + 64;
constexpr int L_K = (L_PART + 4 * 1024 + 255) & ~255;
constexpr int L_END = L_K + 16384;

#define RSRC(p, n) __builtin_amdgcn_make_buffer_rsrc((void*)(p), 0, (n), 0x00020000)
#define BLD128(r, off, soff, aux) __builtin_amdgcn_raw_buffer_load_b128((r), (off), (soff), (aux))
#define BST128(v, r, off, aux) __builtin_amdgcn_raw_buffer_store_b128((v), (r), (off), 0, (aux))
#define CFENCE() asm volatile("" ::: "memory")
#define LDS_WAIT() asm volatile("s_waitcnt lgkmcnt(0)" ::: "memory")
#define KEEP(v) asm volatile("" :: "v"(v))
#define SB() __builtin_amdgcn_sched_barrier(0)
#define LDS_INC(i) __hip_atomic_fetch_add((LAS unsigned*)(flag + (i)), 1u, __ATOMIC_RELAXED, __HIP_MEMORY_SCOPE_WORKGROUP)
#define WBAD(v) (((v)[1] ^ tag) | ((v)[3] ^ tag))
#define PBAD(v) (((v)[0] | (v)[1] | (v)[2] | (v)[3]) & 0x3ffu)
#define QBAD(v) (((v)[0] | (v)[1] | (v)[2] | (v)[3]) & 0xffu)
#define FCHK(k, m) "v_xor_b32 v[24+4*" #k "], %15, v[8+4*" #k "]\n\tv_xor_b32 v[25+4*" #k "], %16, v[9+4*" #k "]\n\t" \
                   "v_xor_b32 v[26+4*" #k "], %15, v[10+4*" #k "]\n\tv_xor_b32 v[27+4*" #k "], %16, v[11+4*" #k "]\n\t" \
                   "v_or3_b32 %9, v[24+4*" #k "], v[25+4*" #k "], v[26+4*" #k "]\n\tv_bitop3_b32 %9, %9, v[27+4*" #k "], %17 bitop3:0xa8\n\tv_cmp_ne_u32 %" #m ", 0, %9\n\t"
constexpr float LOG2E = 1.4426950408889634f;

__device__ __forceinline__ float asf(unsigned u) { return __builtin_bit_cast(float, u); }
__device__ __forceinline__ unsigned asu(float f) { return __builtin_bit_cast(unsigned, f); }
__device__ __forceinline__ unsigned cvtpk(float lo, float hi) {
  bf16x2 r = {(__bf16)lo, (__bf16)hi};
  unsigned u = __builtin_bit_cast(unsigned, r);
  asm("" : "+v"(u));
  return u;
}
__device__ __forceinline__ float lo16(unsigned u) { return asf(u << 16); }
__device__ __forceinline__ float hi16(unsigned u) { return asf(u & 0xffff0000u); }
__device__ __forceinline__ float rb(float f) { return hi16(cvtpk(f, f)); }
template <int CTRL> __device__ __forceinline__ float dpp(float x) {
  return asf(__builtin_amdgcn_mov_dpp(asu(x), CTRL, 0xf, 0xf, true));
}
constexpr int XOR1 = 0xB1, XOR2 = 0x4E, HMIR = 0x141, MIR = 0x140, ROR4 = 0x124, ROR8 = 0x128;
__device__ __forceinline__ float fmx(float a, float b) { float r; asm("v_max_f32 %0, %1, %2" : "=v"(r) : "v"(a), "v"(b)); return r; }
__device__ __forceinline__ float fadd(float a, float b) { float r; asm("v_add_f32 %0, %1, %2" : "=v"(r) : "v"(a), "v"(b)); return r; }
__device__ __forceinline__ float sum8(float x) { x += dpp<XOR1>(x); x += dpp<XOR2>(x); x += dpp<HMIR>(x); return x; }
__device__ __forceinline__ float xrow_sum(float x) {
  auto s = __builtin_amdgcn_permlane16_swap(asu(x), asu(x), false, false);
  x = asf(s[0]) + asf(s[1]);
  auto t = __builtin_amdgcn_permlane32_swap(asu(x), asu(x), false, false);
  return asf(t[0]) + asf(t[1]);
}
__device__ __forceinline__ float xrow_max(float x) {
  auto s = __builtin_amdgcn_permlane16_swap(asu(x), asu(x), false, false);
  x = fmx(asf(s[0]), asf(s[1]));
  auto t = __builtin_amdgcn_permlane32_swap(asu(x), asu(x), false, false);
  return fmx(asf(t[0]), asf(t[1]));
}
__device__ __forceinline__ float xrow_sum4(float v0, float v1, float v2, float v3) {
  auto a = __builtin_amdgcn_permlane32_swap(asu(v0), asu(v2), false, false);
  auto c = __builtin_amdgcn_permlane32_swap(asu(v1), asu(v3), false, false);
  const float p = asf(a[0]) + asf(a[1]), q = asf(c[0]) + asf(c[1]);
  auto e = __builtin_amdgcn_permlane16_swap(asu(p), asu(q), false, false);
  return asf(e[0]) + asf(e[1]);
}
__device__ __forceinline__ float wave_sum(float x) { x = sum8(x); x += dpp<MIR>(x); return xrow_sum(x); }
__device__ __forceinline__ f32x4 mfma(u32x4 a, u32x4 b, f32x4 c) {
  return __builtin_amdgcn_mfma_f32_16x16x32_bf16(__builtin_bit_cast(bf16x8, a), __builtin_bit_cast(bf16x8, b), c, 0, 0, 0);
}
__device__ __forceinline__ unsigned align_hi(float a) { return __builtin_amdgcn_update_dpp(asu(a), asu(a), ROR8, 0xc, 0xf, false); }
template <int NB> __device__ __forceinline__ unsigned pk1(float a, unsigned t) { return ((asu(a) + (1u << (NB - 1))) & (~0u << NB)) | t; }
template <int NB = 10> __device__ __forceinline__ u32x4 pack4(float a, float b, float c, float d, unsigned tag) {
  const unsigned ta = tag & ~(~0u << NB), tb = (tag >> NB) & ~(~0u << NB);
  return u32x4{pk1<NB>(a, ta), pk1<NB>(b, tb), pk1<NB>(c, ta), pk1<NB>(d, tb)};
}
template <int NB = 10> __device__ __forceinline__ u32x4 untag(u32x4 v, unsigned tag) {
  const unsigned ta = tag & ~(~0u << NB), tb = (tag >> NB) & ~(~0u << NB);
  return u32x4{v[0] ^ ta, v[1] ^ tb, v[2] ^ ta, v[3] ^ tb};
}
__device__ __forceinline__ void lds_wait_ge(volatile LAS unsigned* p, unsigned v) {
  _Pragma("nounroll") for (unsigned i = 0; *p < v && i < SPIN_CAP; ++i) __builtin_amdgcn_s_sleep(1);
  CFENCE();
}
__device__ __forceinline__ unsigned now10ns() { return (unsigned)__builtin_amdgcn_s_memrealtime(); }
__device__ __forceinline__ void wait_until(unsigned t0, unsigned ticks) {
  _Pragma("nounroll") while ((int)(now10ns() - t0) < (int)ticks) __builtin_amdgcn_s_sleep(1);
}
__device__ __forceinline__ void pace(unsigned t0, int ticks) {
  asm volatile("1:\n\ts_memrealtime vcc\n\ts_waitcnt lgkmcnt(0)\n\ts_sub_u32 vcc_lo, vcc_lo, %0\n\ts_cmpk_lt_i32 vcc_lo, %1\n\ts_cbranch_scc1 1b"
               :: "s"(t0), "i"(ticks) : "scc", "vcc", "memory");
}
__device__ __forceinline__ void layernorm(float (&v)[12], unsigned (&d)[12], bool act) {
  float s = 0.f;
#pragma unroll
  for (int i = 0; i < 12; i += 4) s += (v[i] + v[i + 1]) + (v[i + 2] + v[i + 3]);
  const float mean = wave_sum(s) * (1.f / D);
  float q0 = 0.f, q1 = 0.f;
#pragma unroll
  for (int i = 0; i < 12; i += 2) { const float a = v[i] - mean, c = v[i + 1] - mean; q0 = fmaf(a, a, q0); q1 = fmaf(c, c, q1); }
  const float var = wave_sum(act ? q0 + q1 : 0.f) * (1.f / D);
  const float mb = rb(mean);
  const float den = rb(__builtin_amdgcn_sqrtf(rb(rb(var) + 1e-5f)));
  const float inv = act ? __builtin_amdgcn_rcpf(den) : 0.f;
#pragma unroll
  for (int i = 0; i < 12; i += 2) {
    const unsigned t = cvtpk(v[i] - mb, v[i + 1] - mb);
    const float a = lo16(t) * inv, c = hi16(t) * inv;
    d[i] = cvtpk(a, a); d[i + 1] = cvtpk(c, c);
    v[i] = hi16(d[i]); v[i + 1] = hi16(d[i + 1]);
  }
}

__global__ void __launch_bounds__(NTHR, 3) fused_k(const u16*, const u16*, const u16*, const u16*, unsigned char*,
                                                   const u16*, const u16*, const u16*, const u16*, const u16*, u16*) {
  u64x8 ka; u64x2 kb; u64 outp;
  asm volatile("s_load_dwordx16 %0, %3, 0x0\n\ts_load_dwordx4 %1, %3, 0x40\n\ts_load_dwordx2 %2, %3, 0x50"
               : "=&s"(ka), "=&s"(kb), "=&s"(outp) : "s"(__builtin_amdgcn_kernarg_segment_ptr()));
  SB();
  __shared__ __attribute__((aligned(16))) unsigned char lds_raw[L_END];
  LAS unsigned char* lds = (LAS unsigned char*)lds_raw;
  volatile LAS unsigned* flag = (volatile LAS unsigned*)(lds + L_FLAG);
  const int b = blockIdx.x, tid = threadIdx.x, w = __builtin_amdgcn_readfirstlane(tid >> 6), l = tid & 63;
  const int g = (b & 7) + 8 * (b >> 7), s = (b >> 3) & 15;
  const int gx = g ^ 1;
  const int jf = (b & 7) * 32 + (b >> 3);
  const int rho = l >> 3, c8 = l & 7, h4 = l >> 4, c16 = l & 15;
  unsigned pf0, pf1;
  {
    u64 pc;
    asm volatile("s_getpc_b64 %2\n\ts_cmp_lg_u32 %3, 6\n\ts_cbranch_scc1 1f\n\t"
                 "v_lshlrev_b32 %0, 7, %4\n\tv_min_u32 %1, %5, %0\n\tv_add_u32 %1, 0x2000, %1\n\t"
                 "global_load_dword %0, %0, %2\n\tglobal_load_dword %1, %1, %2\n1:"
                 : "=&v"(pf0), "=&v"(pf1), "=&s"(pc) : "s"(w + (b & 0xf0)), "v"(l), "s"(CODE_BYTES - 0x2000) : "scc", "memory");
  }
  flag[l] = l < 16 ? 0u : EMPTY;
  const int aw = w - R_ATT;
  int kvb = ((g * SK + (SK - WIN) + s * 256 + aw * 64) * DH * 2) | ((s ^ (int)(((unsigned)((b & 7) * 2 + (b >> 7) + 1) * 0x9E3779B1u) >> 28)) << 10);
  int krec = (unsigned)aw < 4u ? 16 * SK * DH * 2 : 0;
  const int rq = s >> 2, cs = s & 3;
  int l16 = l * 16, l24 = l * 24;
  int base = ((rq * 144 + 4 * rho) * NQ + gx * 256 + cs * 64 + c8 * 8) * 2;
  int base2 = ((rq * 144 + 128 + 2 * rho) * NQ + gx * 256 + cs * 64 + c8 * 8) * 2;
  asm volatile("" : "+v"(l16), "+v"(l24), "+v"(base), "+v"(base2) :: "memory");
  asm volatile("s_waitcnt lgkmcnt(0)" : "+s"(ka), "+s"(kb), "+s"(outp), "+s"(kvb), "+s"(krec) :: "memory");
  const u16 *x = (const u16*)ka[0], *wq = (const u16*)ka[1], *kc = (const u16*)ka[2], *vc = (const u16*)ka[3];
  unsigned char* ws = (unsigned char*)ka[4];
  const u16 *w1 = (const u16*)ka[5], *w2 = (const u16*)ka[6], *wo = (const u16*)ka[7], *b1 = (const u16*)kb[0], *b2 = (const u16*)kb[1];
  u64 t064;
  asm volatile("s_memrealtime %0" : "=s"(t064));
  auto rws = RSRC(ws, WS_END);

  u32x4 R[18], xa; u32x2 xb; unsigned e0;
  auto rk = RSRC(kc, krec);
  const u32x4 K0 = BLD128(rk, l16, kvb, NT);
  SB();
#pragma unroll
  for (int i = 0; i < 18; ++i) asm volatile("" : "=v"(R[i]));
  asm volatile("" : "=v"(xa), "=v"(xb), "=v"(e0));
  if (__builtin_expect(w == 0, 1)) {
    auto rx = RSRC(x, D * 2);
    xa = BLD128(rx, l24, 0, 0);
    xb = __builtin_amdgcn_raw_buffer_load_b64(rx, l24 + 16, 0, 0);
    e0 = __builtin_amdgcn_raw_buffer_load_b32(rws, WS_E, 0, SC1);
    SB();
    auto r = RSRC(wq, D * NQ * 2);
#pragma unroll
    for (int u = 0; u < 16; ++u) R[u] = BLD128(r, base, (32 * (u >> 2) + (u & 3)) * NQ * 2, NT);
    R[16] = BLD128(r, base2, 0, NT); R[17] = BLD128(r, base2, NQ * 2, NT);
  }
  __builtin_amdgcn_s_barrier();
  asm volatile("s_waitcnt lgkmcnt(0)" : "+s"(t064) :: "memory");
  unsigned t0 = (unsigned)t064;
  int w2nd = w;
  asm volatile("" : "+s"(w2nd) :: "memory");
  u32x4 msk;
#pragma unroll
  for (int r = 0; r < 4; ++r) msk[r] = ((c8 >> 1) == r) ? ((c8 & 1) ? 0xffff0000u : 0x0000ffffu) : 0u;
  unsigned tag = 0;
  int job = 0, stag = 0;
  unsigned b1r = 0;
  float xb2 = 0.f;
  const int idx = b >> 3, grp = idx < 16 ? 2 * (b & 7) + (idx >> 3) : 14 + 18 * (b & 7) + (idx >> 3);
  const int oc = grp * 32 + (idx & 7) * 4 + h4;
  const int hf = (18 * jf & ~3) + 4 * c8 + h4;

  if (__builtin_expect((unsigned)(w2nd - R_ATT) < 4u, 1)) {
    u32x4 KV[32], KF[16];
    {
      auto rv = RSRC(vc, 16 * SK * DH * 2);
      LAS unsigned char* kl = lds + L_K + aw * 4096;
      t0 += (2 * aw - 3) * DT_A >> 7;
#pragma unroll
      for (int i = 0; i < 32; ++i) {
        const int j = i - WIN_A;
        if (j >= 0 && j < 16) *(LAS u32x4*)(kl + (4 * h4 + (j & 3)) * 256 + ((c16 ^ (4 * h4 + (j & 3))) << 4)) = KV[j];
        else if (j >= 16) KEEP(KV[j]);
        if (i == 0) KV[0] = K0;
        else KV[i] = BLD128(i < 16 ? rk : rv, l16, kvb ^ ((i & 15) << 10), NT);
        if (i < 31) pace(t0, T_A + (((i + 1) * DT_A) >> 4));
        if (j >= 0 && j < 16 && (j & 3) == 3) {
#pragma unroll
          for (int k = 0; k < 4; ++k) KF[j - 3 + k] = *(LAS u32x4*)(kl + c16 * 256 + (((4 * k + h4) ^ c16) << 4));
        }
        if (i == 30) { if (l == 0) LDS_INC(FL_ISS); }
        if (i == 20) b1r = __builtin_amdgcn_raw_buffer_load_b16(RSRC(b1, F * 2), hf * 2, 0, 0);
        SB();
      }
    }
    CFENCE();
    lds_wait_ge(flag + FL_Q, 1);
    u32x4 Q[4];
#pragma unroll
    for (int t = 0; t < 4; ++t) Q[t] = *(LAS u32x4*)(lds + L_Q + (c16 >> 3) * 256 + t * 64 + h4 * 16);
    tag = __builtin_amdgcn_readfirstlane(flag[FL_TAG]);
    f32x4 S[4];
#pragma unroll
    for (int kt = 0; kt < 4; ++kt) S[kt] = mfma(KF[kt * 4], Q[0], f32x4{0.f, 0.f, 0.f, 0.f});
#pragma unroll
    for (int t = 1; t < 4; ++t)
#pragma unroll
      for (int kt = 0; kt < 4; ++kt) S[kt] = mfma(KF[kt * 4 + t], Q[t], S[kt]);
    float sc[16], m = -INFINITY;
#pragma unroll
    for (int i = 0; i < 16; i += 2) {
      const unsigned t = cvtpk(S[i >> 2][i & 3], S[i >> 2][(i & 3) + 1]);
      const unsigned u = cvtpk(lo16(t) * 0.08838834764831845f, hi16(t) * 0.08838834764831845f);
      sc[i] = lo16(u); sc[i + 1] = hi16(u);
      m = fmaxf(m, fmaxf(sc[i], sc[i + 1]));
    }
    m = xrow_max(m);
    float ls0 = 0.f, ls1 = 0.f;
    unsigned pp[16];
#pragma unroll
    for (int i = 0; i < 16; i += 2) {
      const float p0 = __builtin_amdgcn_exp2f((sc[i] - m) * LOG2E), p1 = __builtin_amdgcn_exp2f((sc[i + 1] - m) * LOG2E);
      ls0 += p0; ls1 += p1; pp[i] = cvtpk(p0, p0); pp[i + 1] = cvtpk(p1, p1);
    }
    const float lsum = xrow_sum(ls0 + ls1);
    if ((l & 55) == 0) *(LAS f32x2*)(lds + L_ML + (rho * 4 + aw) * 8) = f32x2{m, lsum};
    CFENCE();
    if (l == 0) LDS_INC(FL_M);
    f32x4 o0 = {0.f, 0.f, 0.f, 0.f}, o1 = o0;
#pragma unroll
    for (int i = 0; i < 10; i += 2) { o0 = mfma(msk & pp[i], KV[16 + i], o0); o1 = mfma(msk & pp[i + 1], KV[17 + i], o1); }
    lds_wait_ge(flag + FL_M, 4);
    float scale, M, L;
    {
      const f32x4 ma = *(LAS f32x4*)(lds + L_ML + (l >> 5) * 32), mc = *(LAS f32x4*)(lds + L_ML + (l >> 5) * 32 + 16);
      const float mine = *(LAS float*)(lds + L_ML + ((l >> 5) * 4 + aw) * 8);
      M = fmx(fmx(ma[0], ma[2]), fmx(mc[0], mc[2]));
      scale = __builtin_amdgcn_exp2f((mine - M) * LOG2E);
      L = (__builtin_amdgcn_exp2f((ma[0] - M) * LOG2E) * ma[1] + __builtin_amdgcn_exp2f((ma[2] - M) * LOG2E) * ma[3]) +
          (__builtin_amdgcn_exp2f((mc[0] - M) * LOG2E) * mc[1] + __builtin_amdgcn_exp2f((mc[2] - M) * LOG2E) * mc[3]);
      asm volatile("" : "+v"(L));
    }
    const int mo = (l & 28) ? OOB : WS_ML + (g * 16 + s) * 128 + (l >> 5) * 64 + (l & 3) * 16;
    SB();
#pragma unroll
    for (int i = 10; i < 16; i += 2) { o0 = mfma(msk & pp[i], KV[16 + i], o0); o1 = mfma(msk & pp[i + 1], KV[17 + i], o1); }
    *(LAS f32x4*)(lds + L_PART + aw * 1024 + ((h4 >> 1) * 128 + 8 * c16 + 4 * (h4 & 1)) * 4) = (o0 + o1) * scale;
    CFENCE();
    unsigned old = 0;
    if (l == 0) old = LDS_INC(FL_ARR);
    old = __builtin_amdgcn_readfirstlane(old);
    CFENCE();
    if (old == 3) {
      f32x4 pa[4];
#pragma unroll
      for (int i = 0; i < 4; ++i) pa[i] = *(LAS f32x4*)(lds + L_PART + i * 1024 + l * 16);
      const f32x4 o = (pa[0] + pa[1]) + (pa[2] + pa[3]);
      BST128((u32x4{asu(M), tag, asu(L), tag}), rws, mo, SC1);
      BST128(pack4(o[0], o[1], o[2], o[3], tag), rws, WS_OG + (g * 16 + s) * 1024 + l * 16, SC1);
      CFENCE();
    }
    job = old < 2 ? 2 : old == 2 ? 1 : 0;
    stag = old == 0 ? 0 : T_STAG;
  } else if (w2nd == R_W1) {
    const int r64 = (b >> 3) * 2 + (b & 1);
    lds_wait_ge(flag + FL_Y, 1);
    tag = __builtin_amdgcn_readfirstlane(flag[FL_TAG]);
    if (r64 < 54) {
      const int part = r64 / 9, slab = ((((b >> 1) + 1) & 3) * 9) + r64 - part * 9;
      auto r1 = RSRC(w1, D * F * 2);
      const int o1 = ((part * 96 + 24 * h4) * F + slab * 128 + c16 * 8) * 2;
      u32x4 ay[6], W1[24];
#pragma unroll
      for (int i = 0; i < 6; ++i) ay[i] = *(LAS u32x4*)(lds + L_Y + (part * 96 + 24 * h4 + 4 * i) * 4);
      const int ho = l < 32 ? WS_HG + (part * 1152 + slab * 32 + 2 * c16 + h4) * 16 : OOB;
      lds_wait_ge(flag + FL_ISS, 4);
#pragma unroll
      for (int i = 0; i < 24; ++i) {
        W1[i] = BLD128(r1, o1, i * F * 2, NT);
        if (i == 21) flag[FL_W1] = 1u;
        if ((i & 1) && i < 23) pace(t0, T_1 + (((i + 1) * DT_1) >> 6) - (DT_1 >> 6));
        SB();
      }
      f32x4 a0 = {0.f, 0.f, 0.f, 0.f}, a1 = a0;
#pragma unroll
      for (int i = 0; i < 24; i += 2) { a0 = mfma(msk & ay[i >> 2][i & 3], W1[i], a0); a1 = mfma(msk & ay[i >> 2][(i & 3) + 1], W1[i + 1], a1); }
      a0 += a1;
      BST128(pack4(a0[0], a0[1], a0[2], a0[3], tag), rws, ho, SC1);
      CFENCE();
    } else {
      lds_wait_ge(flag + FL_ISS, 4);
      flag[FL_W1] = 1u;
      if (b == 255) {
        _Pragma("nounroll") for (unsigned it = 0; it < SPIN_CAP; ++it) {
          CFENCE();
          unsigned bad = 0;
#pragma unroll
          for (int k = 0; k < 4; ++k) {
            const u32x4 G = untag<8>(BLD128(rws, WS_QG + l * 256, k * 16384, SC1), tag), G2 = untag<8>(BLD128(rws, WS_QG + l * 256 + 16, k * 16384, SC1), tag);
            bad |= QBAD(G) | QBAD(G2);
          }
          if (__all(bad == 0)) break;
          __builtin_amdgcn_s_sleep(16);
        }
        __builtin_amdgcn_raw_buffer_store_b32(tag, rws, l == 0 ? WS_E : OOB, 0, SC1);
      }
    }
  } else if (w2nd >= R_OUT) {
    const int ow = w - R_OUT;
    asm volatile("s_waitcnt vmcnt(0)" : "+v"(pf0), "+v"(pf1) :: "memory");
    auto r2 = RSRC(w2, F * D * 2);
    auto ro = RSRC(wo, NQ * D * 2);
    auto r2b = RSRC(w2 + 64, ow < 4 ? F * D * 2 - 128 : 0);
    auto rob = RSRC(wo + 64, ow < 4 ? NQ * D * 2 - 128 : 0);
    const int col = (ow * 128 + c8 * 8) * 2;
    const int o2 = (18 * jf + 2 * rho) * D * 2 + col;
    const int o2c = rho < 2 ? (18 * jf + 16 + rho) * D * 2 + col : OOB;
    const int oo = (gx * 256 + 16 * s + 2 * rho) * D * 2 + col;
    const int fo = ((l & 8) || (ow == 4 && l >= 32)) ? OOB : WS_FG + ((ow * 4 + (l >> 5) * 2 + (c8 >> 2)) * 256 + b) * 128 + ((c8 & 3) * 2 + (h4 & 1)) * 16;
    lds_wait_ge(flag + FL_W1, 1);
    t0 += (((ow + 1) % 5) * 2 - 3) * DT_W / 80;
    pace(t0, T_W);
    u32x4 W[10];
#pragma unroll
    for (int i = 0; i < 10; ++i) {
      const int u = i >> 1;
      W[i] = u < 2 ? BLD128((i & 1) ? rob : ro, oo + u * D * 2, 0, NT) : BLD128((i & 1) ? r2b : r2, u < 4 ? o2 + (u - 2) * D * 2 : o2c, 0, NT);
      if ((i & 1) && i < 6) pace(t0, T_W + (((i + 1) * DT_W) >> 4));
      SB();
    }
    unsigned ta = flag[FL_TAG], tb = (ta >> 10) & 0x3ffu;
    ta &= 0x3ffu;
    asm volatile("" : "+v"(ta), "+v"(tb) :: "memory");
    u32x2 ao, ah; unsigned ah2;
    _Pragma("nounroll") for (unsigned it = 0; it < SPIN_CAP; ++it) {
      ao = *(volatile LAS u32x2*)(lds + L_AO + rho * 8);
      if (!__any(min(ao[0] ^ EMPTY, ao[1] ^ EMPTY) == 0u)) break;
    }
    f32x4 oc0, oc1;
    { const u32x4 A0 = msk & ao[0], A1 = msk & ao[1];
      oc0 = mfma(A0, W[0], f32x4{0.f, 0.f, 0.f, 0.f}); oc1 = mfma(A0, W[1], f32x4{0.f, 0.f, 0.f, 0.f});
      oc0 = mfma(A1, W[2], oc0); oc1 = mfma(A1, W[3], oc1); }
    _Pragma("nounroll") for (unsigned it = 0; it < SPIN_CAP; ++it) {
      ah = *(volatile LAS u32x2*)(lds + L_AH + rho * 8);
      ah2 = *(volatile LAS unsigned*)(lds + L_AH + 64 + (rho & 1) * 4);
      if (!__any(min(min(ah[0] ^ EMPTY, ah[1] ^ EMPTY), ah2 ^ EMPTY) == 0u)) break;
    }
    { const u32x4 A0 = msk & ah[0], A1 = msk & ah[1], A2 = msk & ah2;
      oc0 = mfma(A0, W[4], oc0); oc1 = mfma(A0, W[5], oc1);
      oc0 = mfma(A1, W[6], oc0); oc1 = mfma(A1, W[7], oc1);
      oc0 = mfma(A2, W[8], oc0); oc1 = mfma(A2, W[9], oc1); }
    float fs[4];
#pragma unroll
    for (int r = 0; r < 4; ++r) {
      auto t = __builtin_amdgcn_permlane32_swap(align_hi(oc0[r]), align_hi(oc1[r]), false, false);
      fs[r] = fadd(asf(t[0]), asf(t[1]));
    }
    BST128((u32x4{pk1<10>(fs[0], ta), pk1<10>(fs[1], tb), pk1<10>(fs[2], ta), pk1<10>(fs[3], tb)}), rws, fo, SC1);
    CFENCE();
    if (l == 0) LDS_INC(FL_OUT);
  } else {
    __builtin_amdgcn_s_setprio(3);
    asm volatile("" : "+v"(xa), "+v"(xb) :: "memory");
    const bool act = l < 48;
    float v[12]; unsigned d[12];
#pragma unroll
    for (int i = 0; i < 4; ++i) { v[2 * i] = lo16(xa[i]); v[2 * i + 1] = hi16(xa[i]); }
#pragma unroll
    for (int i = 0; i < 2; ++i) { v[8 + 2 * i] = lo16(xb[i]); v[9 + 2 * i] = hi16(xb[i]); }
    layernorm(v, d, act);
    if (act) {
#pragma unroll
      for (int i = 0; i < 3; ++i) *(LAS u32x4*)(lds + L_X + l * 48 + 16 * i) = u32x4{d[4 * i], d[4 * i + 1], d[4 * i + 2], d[4 * i + 3]};
    }
    LDS_WAIT();
    u32x4 av[4]; u32x2 av2;
#pragma unroll
    for (int i = 0; i < 4; ++i) av[i] = *(LAS u32x4*)(lds + L_X + (rq * 144 + 32 * i + 4 * rho) * 4);
    av2 = *(LAS u32x2*)(lds + L_X + (rq * 144 + 128 + 2 * rho) * 4);
    f32x4 acc0 = {0.f, 0.f, 0.f, 0.f}, acc1 = acc0;
#pragma unroll
    for (int u = 0; u < 16; u += 2) {
      acc0 = mfma(msk & av[u >> 2][u & 3], R[u], acc0);
      acc1 = mfma(msk & av[u >> 2][(u & 3) + 1], R[u + 1], acc1);
    }
    acc0 = mfma(msk & av2[0], R[16], acc0);
    acc1 = mfma(msk & av2[1], R[17], acc1);
    acc0 += acc1;
    tag = __builtin_amdgcn_readfirstlane(e0) * 1664525u + 1013904223u;
    {
      float qs[4];
#pragma unroll
      for (int r = 0; r < 4; ++r) { const unsigned u = align_hi(acc0[r]); auto t = __builtin_amdgcn_permlane32_swap(u, u, false, false); qs[r] = asf(t[0]) + asf(t[1]); }
      BST128(pack4<8>(qs[0], qs[1], qs[2], qs[3], tag), rws, (l & 40) ? OOB : WS_QG + ((gx * 4 + rq) * 64 + cs * 16 + 2 * c8 + h4) * 16, SC1);
    }
    CFENCE();
    const unsigned tpub = now10ns();
    layernorm(v, d, act);
    if (act) {
#pragma unroll
      for (int i = 0; i < 3; ++i) *(LAS u32x4*)(lds + L_Y + l * 48 + 16 * i) = u32x4{d[4 * i], d[4 * i + 1], d[4 * i + 2], d[4 * i + 3]};
    }
    CFENCE();
    flag[FL_TAG] = tag;
    flag[FL_Y] = 1u;
    wait_until(t0, T_Q);
    wait_until(tpub, T_QPUB);
    {
      u32x4 G[4];
      _Pragma("nounroll") for (unsigned it = 0; it < SPIN_CAP; ++it) {
        CFENCE();
#pragma unroll
        for (int k = 0; k < 4; ++k) G[k] = untag<8>(BLD128(rws, WS_QG + g * 4096 + k * 1024 + l * 16, 0, SC1), tag);
        if (__all((QBAD(G[0]) | QBAD(G[1]) | QBAD(G[2]) | QBAD(G[3])) == 0)) break;
        __builtin_amdgcn_s_sleep(4);
      }
      const f32x4 qv = (__builtin_bit_cast(f32x4, G[0]) + __builtin_bit_cast(f32x4, G[1])) + (__builtin_bit_cast(f32x4, G[2]) + __builtin_bit_cast(f32x4, G[3]));
      *(LAS u32x2*)(lds + L_Q + l * 8) = u32x2{cvtpk(qv[0], qv[1]), cvtpk(qv[2], qv[3])};
    }
    CFENCE();
    flag[FL_Q] = 1u;
    xb2 = lo16(__builtin_amdgcn_raw_buffer_load_b16(RSRC(x, D * 2), oc * 2, 0, 0)) + lo16(__builtin_amdgcn_raw_buffer_load_b16(RSRC(b2, D * 2), oc * 2, 0, 0));
    asm volatile("" : "+v"(xb2));
    job = 5;
  }

  if (job & 1) {
    const int og = WS_OG + (gx * 16 + (l >> 2)) * 1024 + s * 64 + (l & 3) * 16;
    const int mg = WS_ML + (gx * 16 + (l >> 2)) * 128 + (s >> 3) * 64;
    wait_until(t0, T_O + stag);
    _Pragma("nounroll") for (unsigned it = 0; it < SPIN_CAP; ++it) {
      CFENCE();
      if (*(volatile LAS unsigned*)(lds + L_AO) != EMPTY) break;
      const u32x4 O = untag(BLD128(rws, og, 0, SC1), tag), ML = BLD128(rws, mg, 0, SC1);
      if (__all((PBAD(O) | WBAD(ML)) == 0)) {
        const float m = asf(ML[0]);
        float M = fmx(m, dpp<ROR4>(m)); M = fmx(M, dpp<ROR8>(M)); M = xrow_max(M);
        const float e = __builtin_amdgcn_exp2f((m - M) * LOG2E);
        const f32x4 o = __builtin_bit_cast(f32x4, O) * e;
        float L = e * asf(ML[2]);
        float n = xrow_sum4(o[0], o[1], o[2], o[3]);
        L = xrow_sum(L);
        n += dpp<ROR8>(n); L += dpp<ROR8>(L);
        n += dpp<ROR4>(n); L += dpp<ROR4>(L);
        n *= __builtin_amdgcn_rcpf(L);
        *(LAS unsigned*)(lds + L_AO + ((l & 3) * 4 + h4) * 4) = cvtpk(n, n);
        break;
      }
      __builtin_amdgcn_s_sleep(1);
    }
  }
  if (job & 2) {
    const int fi = hf - 18 * jf;
    const bool use = rho < 6 && c8 < 5;
    const int hg = use ? WS_HG + (rho * 1152 + (18 * jf >> 2) + c8) * 16 : OOB;
    const unsigned ltag = use ? tag : 0u;
    float b1v = lo16(b1r);
    int ha = L_AH + ((unsigned)fi < 18u ? fi : 31) * 4;
    asm volatile("" : "+v"(b1v), "+v"(ha));
    wait_until(t0, T_H + stag);
    _Pragma("nounroll") for (unsigned it = 0; it < SPIN_CAP; ++it) {
      CFENCE();
      if (*(volatile LAS unsigned*)(lds + L_AH) != EMPTY) break;
      const u32x4 H = untag(BLD128(rws, hg, 0, SC1), ltag);
      if (__all(PBAD(H) == 0)) {
        const f32x4 hv = __builtin_bit_cast(f32x4, H);
        float t = xrow_sum4(hv[0], hv[1], hv[2], hv[3]);
        t += dpp<ROR8>(t);
        const float a = rb(rb(t) + b1v);
        constexpr float C1 = -2.f * 0.7978845608028654f * LOG2E, C3 = C1 * 0.044715f;
        const float hh = a * __builtin_amdgcn_rcpf(1.f + __builtin_amdgcn_exp2f(a * fmaf(a * a, C3, C1)));
        *(LAS unsigned*)(lds + ha) = cvtpk(hh, hh);
        break;
      }
      __builtin_amdgcn_s_sleep(1);
    }
  }
  if (job & 4) {
    if (grp < 18) {
      int fo = WS_FG + (grp * 256 + l) * 128 + (idx & 7) * 16, fo1 = fo + 8192, fo2 = fo + 16384, fo3 = fo + 24576;
      int oo = c16 == 0 ? oc * 2 : OOB;
      asm volatile("" : "+v"(fo), "+v"(fo1), "+v"(fo2), "+v"(fo3), "+v"(xb2), "+v"(oo));
      auto ro = RSRC(outp, D * 2);
      lds_wait_ge(flag + FL_OUT, 5);
      __builtin_amdgcn_s_sleep(10);
      f32x4 P[4];
      {
        const u32x4 rd = {(unsigned)ka[4], (unsigned)(ka[4] >> 32) & 0xffffu, (unsigned)WS_END, 0x00020000u};
        u64 m0 = ~0ull, m1 = ~0ull, m2 = ~0ull, m3 = ~0ull; unsigned n = SPIN_CAP, t;
        asm volatile(
            "1:\n\t"
            "s_mov_b64 exec, %4\n\tbuffer_load_dwordx4 v[8:11], %10, %14, 0 offen sc1\n\t"
            "s_mov_b64 exec, %5\n\tbuffer_load_dwordx4 v[12:15], %11, %14, 0 offen sc1\n\t"
            "s_mov_b64 exec, %6\n\tbuffer_load_dwordx4 v[16:19], %12, %14, 0 offen sc1\n\t"
            "s_mov_b64 exec, %7\n\tbuffer_load_dwordx4 v[20:23], %13, %14, 0 offen sc1\n\t"
            "s_mov_b64 exec, -1\n\t"
            "s_sub_u32 %8, %8, 1\n\ts_waitcnt vmcnt(0)\n\ts_cbranch_scc1 2f\n\t"
            FCHK(0, 4) FCHK(1, 5) FCHK(2, 6) FCHK(3, 7)
            "s_or_b64 vcc, %4, %5\n\ts_or_b64 vcc, vcc, %6\n\ts_nop 0\n\ts_or_b64 vcc, vcc, %7\n\t"
            "s_cbranch_scc1 1b\n"
            "2:"
            : "=&{v[24:27]}"(P[0]), "=&{v[28:31]}"(P[1]), "=&{v[32:35]}"(P[2]), "=&{v[36:39]}"(P[3]), "+s"(m0), "+s"(m1), "+s"(m2), "+s"(m3), "+s"(n), "=&v"(t)
            : "v"(fo), "v"(fo1), "v"(fo2), "v"(fo3), "s"(rd), "s"(tag & 0x3ffu), "s"((tag >> 10) & 0x3ffu), "s"(0x3ffu)
            : "vcc", "scc", "memory", "v8", "v9", "v10", "v11", "v12", "v13", "v14", "v15", "v16", "v17", "v18", "v19", "v20", "v21", "v22", "v23");
      }
      const f32x4 t = (P[0] + P[1]) + (P[2] + P[3]);
      float r = xrow_sum4(t[0], t[1], t[2], t[3]);
      r = sum8(r); r += dpp<MIR>(r);
      __builtin_amdgcn_raw_buffer_store_b16((u16)cvtpk(r + xb2, r + xb2), ro, oo, 0, 0);
    }
  }
}

extern "C" void kernel_launch(void* const* d_in, const int* in_sizes, int n_in, void* d_out, int out_size, void* d_ws, size_t ws_size,
                              hipStream_t stream) {
  const u16* x = (const u16*)d_in[0];
  const u16* kc = (const u16*)d_in[1];
  const u16* vc = (const u16*)d_in[2];
  const u16* wq = (const u16*)d_in[3];
  const u16* wo = (const u16*)d_in[4];
  const u16* w1 = (const u16*)d_in[6];
  const u16* b1 = (const u16*)d_in[7];
  const u16* w2 = (const u16*)d_in[8];
  const u16* b2 = (const u16*)d_in[9];
  fused_k<<<256, NTHR, 0, stream>>>(x, wq, kc, vc, (unsigned char*)d_ws, w1, w2, wo, b1, b2, (u16*)d_out);
}
```
